```python
import torch

def _softmax(x, axis=-1):
    return torch.softmax(x, dim=axis)

def _gelu(x):
    return torch.nn.functional.gelu(x, approximate='tanh')

def _silu(x):
    return torch.nn.functional.silu(x)

def _sigmoid(x):
    return torch.sigmoid(x)

def _layernorm(x, axis=-1, eps=1e-05):
    m = x.mean(dim=axis, keepdim=True)
    v = x.var(dim=axis, keepdim=True, unbiased=False)
    return (x - m) / torch.sqrt(v + eps)

def _scatter_add(tgt, idx, src, axis=0):
    return tgt.clone().index_add_(axis, idx, src)

def _segment_sum(src, seg, n):
    out = torch.zeros((n,) + src.shape[1:], dtype=src.dtype)
    return out.index_add_(0, seg, src)

def _pad(x, pw):
    flat = tuple((v for pair in reversed(pw) for v in pair))
    return torch.nn.functional.pad(x, flat)
kernel_name = 'adaLN_cond__ssm_conv_h3__dense_activation_silu__prompt'

def setup_inputs():

    _w = lambda a: (lambda t: t.to(torch.bfloat16) if t.is_floating_point() else t)(torch.as_tensor(a))

    def _A_setup():
        B, S, D = (1, 2048, 1024)
        ks = 4
        return {'q': _w((lambda *s: torch.randn(*s, dtype=torch.bfloat16) * 0.05)(B, S, D)), 'k': _w((lambda *s: torch.randn(*s, dtype=torch.bfloat16) * 0.8)(B, S, D)), 'v': _w((lambda *s: torch.randn(*s, dtype=torch.bfloat16) * 0.8)(B, S, D)), 'shift_k': _w((lambda *s: torch.randn(*s, dtype=torch.bfloat16) * 0.8)(D, ks) * 0.5), 'ssm_k': _w((lambda *s: torch.randn(*s, dtype=torch.bfloat16) * 0.8)(D, S) * (1.0 / S ** 0.5)), 'ssm_D': _w((lambda *s: torch.randn(*s, dtype=torch.bfloat16) * 0.8)(D))}
    _A_in = _A_setup()

    def _B_setup():
        B, S, D, F = (1, 2048, 1024, 3584)
        return {'x': _w((lambda *s: torch.randn(*s, dtype=torch.bfloat16) * 0.05)(B, S, D)), 'w1': _w((lambda *s: torch.randn(*s, dtype=torch.bfloat16) * 3.2)(D, F) * D ** (-0.5)), 'w2': _w((lambda *s: torch.randn(*s, dtype=torch.bfloat16) * 1.6)(F, D) * F ** (-0.5))}
    _B_in = _B_setup()
    return {'A_q': _A_in['q'], 'A_k': _A_in['k'], 'A_v': _A_in['v'], 'A_shift_k': _A_in['shift_k'], 'A_ssm_k': _A_in['ssm_k'], 'A_ssm_D': _A_in['ssm_D'], 'B_x': _B_in['x'], 'B_w1': _B_in['w1'], 'B_w2': _B_in['w2'], 'c': _w((lambda *s: torch.randn(*s, dtype=torch.bfloat16) * 0.1)(1, 1024) * 8.0), 'w_mod': _w((lambda *s: torch.randn(*s, dtype=torch.bfloat16) * 0.1)(1024, 6 * 1024) * 1024 ** (-0.5) * 8.0)}

def reference(A_q, A_k, A_v, A_shift_k, A_ssm_k, A_ssm_D, B_x, B_w1, B_w2, c, w_mod):

    def _A_ref(q, k, v, shift_k, ssm_k, ssm_D):
        B, S, D = q.shape
        ks = shift_k.shape[-1]
        k_pad = _pad(k, ((0, 0), (ks - 1, 0), (0, 0)))
        k_shift = torch.zeros_like(k)
        for t in range(ks):
            k_shift = k_shift + k_pad[:, t:t + S, :] * shift_k[:, ks - 1 - t][None, None, :]
        kv = k_shift * v
        L = 2 * S
        kv_pad = _pad(kv, ((0, 0), (0, S), (0, 0)))
        kf_pad = _pad(ssm_k, ((0, 0), (0, S)))
        kv_f = (lambda x, axes=(-2, -1): torch.fft.rfft2(x.float(), dim=axes))(kv_pad[:, None, :, :], axes=(1, 2))
        kf_f = (lambda x, axes=(-2, -1): torch.fft.rfft2(x.float(), dim=axes))(kf_pad[:, None, :], axes=(1, 2))
        kf_f = (lambda x, *d: x.permute(*d))(kf_f, 1, 2, 0)
        y_f = kv_f * kf_f[None, :, :, :]
        y = (lambda x, s, axes=(-2, -1): torch.fft.irfft2(x, s=s, dim=axes).to(torch.bfloat16))(y_f, (1, L), axes=(1, 2))[:, 0, :S, :]
        y = y + kv * ssm_D[None, None, :]
        return q * y

    def _B_ref(x, w1, w2):
        h = torch.einsum('bsd,df->bsf', x, w1)
        h = _silu(h)
        out = torch.einsum('bsf,fd->bsd', h, w2)
        return out
    x = A_q
    mod = (_silu(c) @ w_mod).reshape(c.shape[0], 6, 1, -1)
    sa, ca, ga, sb, cb, gb = (mod[:, 0].clone(), mod[:, 1].clone(), mod[:, 2].clone(), mod[:, 3].clone(), mod[:, 4].clone(), mod[:, 5].clone())
    h = _layernorm(x, axis=-1) * (1 + ca) + sa
    x = x + ga * _A_ref(h, A_k, A_v, A_shift_k, A_ssm_k, A_ssm_D)
    h = _layernorm(x, axis=-1) * (1 + cb) + sb
    x = x + gb * _B_ref(h, B_w1, B_w2)
    return x
```

```cpp
#include <hip/hip_runtime.h>
#include <hip/hip_bf16.h>
typedef unsigned short u16;
constexpr int S=2048, D=1024, F=3584;
__device__ __forceinline__ float bf2f(u16 x){ return __uint_as_float(((unsigned)x)<<16); }
__device__ __forceinline__ u16 f2bf(float f){ __hip_bfloat16 h = __float2bfloat16(f); return *reinterpret_cast<u16*>(&h); }
__device__ __forceinline__ float rbf(float f){ return bf2f(f2bf(f)); }

__global__ void k_mod(const u16* c, const u16* wm, float* mod){
  int j = blockIdx.x*256+threadIdx.x; float a=0;
  for(int i=0;i<1024;i++){ float x=bf2f(c[i]); float s=rbf(x/(1.f+__expf(-x))); a+=s*bf2f(wm[(size_t)i*6144+j]); }
  mod[j]=rbf(a);
}
__global__ void k_kv(const u16* k,const u16* v,const u16* sk,u16* kv){
  int idx=blockIdx.x*256+threadIdx.x; int s=idx/D,d=idx%D; float a=0;
  for(int t=0;t<4;t++){ int ss=s+t-3; float kk= ss>=0? bf2f(k[(size_t)ss*D+d]):0.f; a=rbf(a+rbf(kk*bf2f(sk[d*4+3-t]))); }
  kv[idx]=f2bf(a*bf2f(v[idx]));
}
__global__ void k_conv(const u16* kv,const u16* sk,const u16* Dv,u16* y){
  int idx=blockIdx.x*256+threadIdx.x; int s=idx/D,d=idx%D; float a=0;
  for(int j=0;j<=s;j++) a+=bf2f(kv[(size_t)j*D+d])*bf2f(sk[(size_t)d*S+s-j]);
  y[idx]=f2bf(rbf(a)+rbf(bf2f(kv[idx])*bf2f(Dv[d])));
}
__device__ float blocksum(float x,float* sh){
  for(int o=32;o>0;o>>=1) x+=__shfl_xor(x,o);
  __syncthreads(); if((threadIdx.x&63)==0) sh[threadIdx.x>>6]=x; __syncthreads();
  return sh[0]+sh[1]+sh[2]+sh[3];
}
__global__ void k_row(const u16* q,const u16* y,const float* mod,u16* x1,u16* h2){
  __shared__ float sh[4]; int s=blockIdx.x; float x[4],z[4];
  float sum=0; for(int i=0;i<4;i++){ x[i]=bf2f(q[(size_t)s*D+threadIdx.x*4+i]); sum+=x[i]; }
  float m=blocksum(sum,sh)/D; float vs=0; for(int i=0;i<4;i++){ float t=x[i]-m; vs+=t*t; }
  float var=rbf(blocksum(vs,sh)/D); m=rbf(m); float sd=rbf(sqrtf(rbf(var+1e-5f)));
  sum=0;
  for(int i=0;i<4;i++){ int d=threadIdx.x*4+i; float h=rbf(rbf(rbf(rbf(x[i]-m)/sd)*rbf(1.f+mod[D+d]))+mod[d]);
    float r=rbf(h*bf2f(y[(size_t)s*D+d])); z[i]=rbf(x[i]+rbf(mod[2*D+d]*r)); sum+=z[i]; x1[(size_t)s*D+d]=f2bf(z[i]); }
  m=blocksum(sum,sh)/D; vs=0; for(int i=0;i<4;i++){ float t=z[i]-m; vs+=t*t; }
  var=rbf(blocksum(vs,sh)/D); m=rbf(m); sd=rbf(sqrtf(rbf(var+1e-5f)));
  for(int i=0;i<4;i++){ int d=threadIdx.x*4+i; h2[(size_t)s*D+d]=f2bf(rbf(rbf(rbf(z[i]-m)/sd)*rbf(1.f+mod[4*D+d]))+mod[3*D+d]); }
}
__global__ void k_g1(const u16* h2,const u16* w1,u16* H){
  int f=blockIdx.x*256+threadIdx.x; int s=blockIdx.y; float a=0;
  for(int k=0;k<D;k++) a+=bf2f(h2[(size_t)s*D+k])*bf2f(w1[(size_t)k*F+f]);
  a=rbf(a); H[(size_t)s*F+f]=f2bf(a/(1.f+__expf(-a)));
}
__global__ void k_g2(const u16* H,const u16* w2,const u16* x1,const float* mod,u16* out){
  int d=blockIdx.x*256+threadIdx.x; int s=blockIdx.y; float a=0;
  for(int k=0;k<F;k++) a+=bf2f(H[(size_t)s*F+k])*bf2f(w2[(size_t)k*D+d]);
  out[(size_t)s*D+d]=f2bf(bf2f(x1[(size_t)s*D+d])+rbf(mod[5*D+d]*rbf(a)));
}
extern "C" void kernel_launch(void* const* d_in, const int* in_sizes, int n_in, void* d_out, int out_size, void* d_ws, size_t ws_size, hipStream_t stream){
  const u16* q=(const u16*)d_in[0]; const u16* k=(const u16*)d_in[1]; const u16* v=(const u16*)d_in[2];
  const u16* shk=(const u16*)d_in[3]; const u16* ssmk=(const u16*)d_in[4]; const u16* ssmD=(const u16*)d_in[5];
  const u16* w1=(const u16*)d_in[7]; const u16* w2=(const u16*)d_in[8]; const u16* c=(const u16*)d_in[9]; const u16* wm=(const u16*)d_in[10];
  char* ws=(char*)d_ws; float* mod=(float*)ws; u16* kv=(u16*)(ws+(1<<20)); u16* y=kv+S*D; u16* x1=y+S*D; u16* h2=x1+S*D; u16* H=h2+S*D;
  k_mod<<<24,256,0,stream>>>(c,wm,mod);
  k_kv<<<S*D/256,256,0,stream>>>(k,v,shk,kv);
  k_conv<<<S*D/256,256,0,stream>>>(kv,ssmk,ssmD,y);
  k_row<<<S,256,0,stream>>>(q,y,mod,x1,h2);
  k_g1<<<dim3(F/256,S),256,0,stream>>>(h2,w1,H);
  k_g2<<<dim3(D/256,S),256,0,stream>>>(H,w2,x1,mod,(u16*)d_out);
}
```

```cpp
#include <hip/hip_runtime.h>
#include <hip/hip_bf16.h>
typedef unsigned short u16;
typedef unsigned long long u64;
typedef __bf16 bf16x2 __attribute__((ext_vector_type(2)));
typedef float f32x2 __attribute__((ext_vector_type(2)));
typedef float f32x4 __attribute__((ext_vector_type(4)));
typedef unsigned u32x2 __attribute__((ext_vector_type(2)));
typedef unsigned u32x4 __attribute__((ext_vector_type(4)));
#define LAS __attribute__((address_space(3)))
#define GAS __attribute__((address_space(1)))
#define DI __device__ __forceinline__
constexpr int S_=2048, D_=1024, F_=3584;
constexpr unsigned OFF_FLAG=0, OFF_MOD=65536, OFF_UT=1u<<20, OFF_YX=OFF_UT+(4u<<20), OFF_H2=OFF_YX+(4u<<20), OFF_X1=OFF_H2+(4u<<20),
                   OFF_XCH=OFF_X1+(4u<<20), OFF_HT=OFF_XCH+(8u<<20), WS_END=OFF_HT+S_*F_*2;
constexpr int SC1=16, NT=2;
typedef __amdgpu_buffer_rsrc_t rsrc_t;
DI rsrc_t mkrs(const void* p, unsigned n){ return __builtin_amdgcn_make_buffer_rsrc((void*)p,0,n,0x00020000); }
DI unsigned cvtpk(float lo, float hi){ f32x2 v={lo,hi}; bf16x2 b=__builtin_convertvector(v,bf16x2); return __builtin_bit_cast(unsigned,b); }
DI float bflo(unsigned u){ return __uint_as_float(u<<16); }
DI float bfhi(unsigned u){ return __uint_as_float(u&0xffff0000u); }
DI float rbf(float x){ return bflo(cvtpk(x,x)); }
DI float silu_f(float x){ return x*__builtin_amdgcn_rcpf(1.f+__builtin_amdgcn_exp2f(x*-1.44269504089f)); }
template<int AUX> DI void bl_lds(rsrc_t rs, unsigned ldsoff, unsigned voff, unsigned soff){
  __builtin_amdgcn_raw_ptr_buffer_load_lds(rs,(LAS void*)ldsoff,16,voff,soff,0,AUX); }
template<int AUX> DI u32x4 bl128(rsrc_t rs, unsigned voff, unsigned soff){ return __builtin_amdgcn_raw_buffer_load_b128(rs,voff,soff,AUX); }
template<int AUX> DI u32x2 bl64(rsrc_t rs, unsigned voff, unsigned soff){ return __builtin_amdgcn_raw_buffer_load_b64(rs,voff,soff,AUX); }
template<int AUX> DI void bs128(u32x4 v, rsrc_t rs, unsigned voff, unsigned soff){ __builtin_amdgcn_raw_buffer_store_b128(v,rs,voff,soff,AUX); }
template<int AUX> DI void bs64(u32x2 v, rsrc_t rs, unsigned voff, unsigned soff){ __builtin_amdgcn_raw_buffer_store_b64(v,rs,voff,soff,AUX); }
#define MF16(acc,a,b) asm volatile("v_mfma_f32_16x16x32_bf16 %0, %1, %2, %0" : "+v"(acc) : "v"(a), "v"(b))
#define RD128(d,a,o) asm volatile("ds_read_b128 %0, %1 offset:%2" : "=v"(d) : "v"(a), "i"(o))
#define RDTR(d,a,o)  asm volatile("ds_read_b64_tr_b16 %0, %1 offset:%2" : "=v"(d) : "v"(a), "i"(o))
#define RD2(d,a,o0,o1) asm volatile("ds_read2_b32 %0, %1 offset0:%2 offset1:%3" : "=v"(d) : "v"(a), "i"(o0), "i"(o1))
#define LGKM(n) asm volatile("s_waitcnt lgkmcnt(%0)"::"i"(n):"memory")
#define VMC(n) asm volatile("s_waitcnt vmcnt(%0)"::"i"(n):"memory")
#define BAR() asm volatile("s_barrier":::"memory")
#define BAR_L() asm volatile("s_waitcnt lgkmcnt(0)\n\ts_barrier":::"memory")
#define BAR_V(n) asm volatile("s_waitcnt vmcnt(%0)\n\ts_barrier"::"i"(n):"memory")
#define BAR_VL(n) asm volatile("s_waitcnt vmcnt(%0) lgkmcnt(0)\n\ts_barrier"::"i"(n):"memory")
#define MFMA_DRAIN() asm volatile("s_nop 7\n\ts_nop 7":::"memory")
DI u32x4 cat(u32x2 a,u32x2 b){ return (u32x4){a.x,a.y,b.x,b.y}; }
template<class T> DI T ldsr(unsigned a){ return *(LAS T*)a; }
template<class T> DI void ldsw(unsigned a, T v){ *(LAS T*)a=v; }
DI float xsum8_16_32(float x){
  x+=__builtin_bit_cast(float,__builtin_amdgcn_update_dpp(0u,__builtin_bit_cast(unsigned,x),0x128,0xf,0xf,false));
  { unsigned u=__builtin_bit_cast(unsigned,x); auto r=__builtin_amdgcn_permlane16_swap(u,u,false,false); const unsigned a=r[0],c=r[1]; x=__uint_as_float(a)+__uint_as_float(c); }
  { unsigned u=__builtin_bit_cast(unsigned,x); auto r=__builtin_amdgcn_permlane32_swap(u,u,false,false); const unsigned a=r[0],c=r[1]; x=__uint_as_float(a)+__uint_as_float(c); }
  return x; }

__global__ __launch_bounds__(512) void k_p0(const u16* k,const u16* v,const u16* shk,char* ws){
  const int tid=threadIdx.x, wid=__builtin_amdgcn_readfirstlane(tid>>6), lane=tid&63, b=blockIdx.x;
  const int dblk=b&7, s0=(b>>3)*64;
  if (tid<64) ((GAS unsigned*)(ws+OFF_FLAG))[b*64+tid]=0;
  rsrc_t rk=mkrs(k,S_*D_*2), rv=mkrs(v,S_*D_*2);
  const int pos=lane&15, lr=lane>>4;
  #pragma unroll
  for(int z=0;z<3;z++){ const int i=wid+8*z; if(i<17){ int rho=4*i+lr; int f=(rho&3)|((rho>>3&1)<<2); int c=(((pos>>1)^f)<<1)|(pos&1);
      bl_lds<NT>(rk,i*1024,(s0-4+rho)*2048+dblk*256+c*16,0);
      if(z<2) bl_lds<NT>(rv,17408+i*1024,(s0+rho)*2048+dblk*256+c*16,0); } }
  const int ch=dblk*128+wid*16+pos;
  u32x2 tp=*(const GAS u32x2*)(shk+ch*4);
  BAR_V(0);
  const float w0=bflo(tp.x),w1=bfhi(tp.x),w2=bflo(tp.y),w3=bfhi(tp.y);
  const int q=pos>>2,p=pos&3,par=(lr&1)<<2;
  const unsigned a0=(8*lr+q)*256+((wid^(q|par))*32)+p*8, a1=(8*lr+q)*256+((wid^(q|(par^4)))*32)+p*8;
  u32x2 kk[2][3],vv[2][2];
  #pragma unroll
  for(int z=0;z<2;z++){ RDTR(kk[z][0],a0,z*8192); RDTR(kk[z][1],a0,z*8192+1024); RDTR(kk[z][2],a1,z*8192+2048); RDTR(vv[z][0],a0,17408+z*8192); RDTR(vv[z][1],a0,17408+z*8192+1024); }
  asm volatile("s_waitcnt lgkmcnt(0)":"+v"(kk[0][0]),"+v"(kk[0][1]),"+v"(kk[0][2]),"+v"(kk[1][0]),"+v"(kk[1][1]),"+v"(kk[1][2]),"+v"(vv[0][0]),"+v"(vv[0][1]),"+v"(vv[1][0]),"+v"(vv[1][1])::"memory");
  rsrc_t ru=mkrs(ws+OFF_UT,S_*D_*2);
  #pragma unroll
  for(int z=0;z<2;z++){
    float kf[12],vf[8],u[8];
    #pragma unroll
    for(int j=0;j<3;j++){ kf[4*j]=bflo(kk[z][j].x); kf[4*j+1]=bfhi(kk[z][j].x); kf[4*j+2]=bflo(kk[z][j].y); kf[4*j+3]=bfhi(kk[z][j].y); }
    #pragma unroll
    for(int j=0;j<2;j++){ vf[4*j]=bflo(vv[z][j].x); vf[4*j+1]=bfhi(vv[z][j].x); vf[4*j+2]=bflo(vv[z][j].y); vf[4*j+3]=bfhi(vv[z][j].y); }
    #pragma unroll
    for(int i=0;i<8;i++) u[i]=(kf[i+1]*w3+kf[i+2]*w2+kf[i+3]*w1+kf[i+4]*w0)*vf[i];
    bs128<SC1>((u32x4){cvtpk(u[7],u[6]),cvtpk(u[5],u[4]),cvtpk(u[3],u[2]),cvtpk(u[1],u[0])},ru,ch*4096+(s0+8*(lr+4*z))*2,0);
  }
}

constexpr int CH_U=0, CH_C0=4608, CH_C1=CH_C0+4160, CH_STR=CH_C1+4160;
constexpr int P1_SC=4*CH_STR, P1_RED=P1_SC+4096, P1_LDS=P1_RED+1024;
__global__ __launch_bounds__(512,2) void k_p1(const u16* ssmk,const u16* ssmD,const u16* cvec,const u16* wmod,char* ws){
  const int tid=threadIdx.x, wid=__builtin_amdgcn_readfirstlane(tid>>6), lane=tid&63, b=blockIdx.x;
  const int w=wid&3, ch=b*4+w; const unsigned cb=w*CH_STR;
  if (wid>=4){
    rsrc_t rh=mkrs(ssmk,S_*D_*2);
    u32x4 hv[4]; unsigned hn[4];
    #pragma unroll
    for(int i=0;i<4;i++){ hv[i]=bl128<NT>(rh,ch*4096+i*1024+lane*16,0); hn[i]=__builtin_amdgcn_raw_buffer_load_b32(rh,ch*4096+i*1024+lane*16+16,0,NT); }
    u32x4 wv[32]; u32x2 cv;
    const bool gv=b<96; rsrc_t rw=mkrs(wmod,gv? 1024*6144*2 : 0);
    cv=*(const GAS u32x2*)(cvec+w*256+lane*4);
    #pragma unroll
    for(int i=0;i<32;i++) wv[i]=bl128<NT>(rw,(w*256+(lane>>3))*12288+b*128+(lane&7)*16,i*8*12288);
    #pragma unroll
    for(int i=0;i<4;i++){ const unsigned a=cb+32+i*1024+lane*16;
      ldsw<u32x4>(a+CH_C0,hv[i]);
      ldsw<u32x4>(a+CH_C1,(u32x4){__builtin_amdgcn_alignbit(hv[i].y,hv[i].x,16),__builtin_amdgcn_alignbit(hv[i].z,hv[i].y,16),__builtin_amdgcn_alignbit(hv[i].w,hv[i].z,16),__builtin_amdgcn_alignbit(hn[i],hv[i].w,16)}); }
    if(lane<8){ ldsw<unsigned>(cb+CH_C0+lane*4,0u); }
    { unsigned h0=__builtin_amdgcn_readfirstlane(hv[0].x); if(lane<8) ldsw<unsigned>(cb+CH_C1+lane*4, lane==7? h0<<16 : 0u); }
    BAR_L();
    if(gv){
      ldsw<f32x4>(P1_SC+w*1024+lane*16,(f32x4){rbf(silu_f(bflo(cv.x))),rbf(silu_f(bfhi(cv.x))),rbf(silu_f(bflo(cv.y))),rbf(silu_f(bfhi(cv.y)))});
      float a[8]={0,0,0,0,0,0,0,0};
      #pragma unroll
      for(int i=0;i<32;i++){ float s=ldsr<float>(P1_SC+w*1024+(i*8+(lane>>3))*4);
        a[0]+=s*bflo(wv[i].x); a[1]+=s*bfhi(wv[i].x); a[2]+=s*bflo(wv[i].y); a[3]+=s*bfhi(wv[i].y); a[4]+=s*bflo(wv[i].z); a[5]+=s*bfhi(wv[i].z); a[6]+=s*bflo(wv[i].w); a[7]+=s*bfhi(wv[i].w); }
      #pragma unroll
      for(int j=0;j<8;j++) a[j]=xsum8_16_32(a[j]);
      if(lane<8){ ldsw<f32x4>(P1_RED+w*256+lane*32,(f32x4){a[0],a[1],a[2],a[3]}); ldsw<f32x4>(P1_RED+w*256+lane*32+16,(f32x4){a[4],a[5],a[6],a[7]}); }
    }
    BAR_L();
    if(gv && wid==4 && lane<32){ f32x2 s=ldsr<f32x2>(P1_RED+lane*8)+ldsr<f32x2>(P1_RED+256+lane*8)+ldsr<f32x2>(P1_RED+512+lane*8)+ldsr<f32x2>(P1_RED+768+lane*8);
      __builtin_amdgcn_raw_buffer_store_b32(cvtpk(s.x,s.y),mkrs(ws+OFF_MOD,12288),b*128+lane*4,0,SC1); }
  } else {
    rsrc_t ru=mkrs(ws+OFF_UT,S_*D_*2);
    #pragma unroll
    for(int i=0;i<4;i++) bl_lds<SC1>(ru,cb+CH_U+512+i*1024,ch*4096+i*1024+lane*16,0);
    if(lane<32) ldsw<u32x4>(cb+CH_U+lane*16,(u32x4){0,0,0,0});
    const float Dv=bflo(ssmD[ch]);
    const int r=lane&15,G=lane>>4;
    const unsigned aB=cb+CH_U+32+32*r+16*G;
    const unsigned aA0=cb+((r&1)? CH_C0+2*(r+25-8*G) : CH_C1+2*(r+24-8*G));
    unsigned aA[4]={aA0,aA0+1024,aA0+2048,aA0+3072};
    f32x4 acc[8];
    #pragma unroll
    for(int n=0;n<8;n++){ acc[n]=(f32x4){0,0,0,0}; asm volatile("":"+v"(acc[n])); }
    BAR_VL(0);
    u32x2 A[2][8][2]; u32x4 B[2][8];
    #define RDA(bf,pp,qq) do{ RD2(A[bf][qq][0],aA[(qq)>>1],16*(pp)+128*((qq)&1),16*(pp)+128*((qq)&1)+1); RD2(A[bf][qq][1],aA[(qq)>>1],16*(pp)+128*((qq)&1)+2,16*(pp)+128*((qq)&1)+3); }while(0)
    #define RDB(bf,pp,mm) RD128(B[bf][mm],aB,512*(mm)-64*(pp)+448)
    #pragma unroll
    for(int i=0;i<8;i++){ RDA(0,0,i); RDB(0,0,i); }
    #pragma unroll
    for(int pp=0;pp<8;pp++){
      const int cu=pp&1, nx=cu^1;
      asm volatile("s_waitcnt lgkmcnt(0)":"+v"(B[cu][0]),"+v"(B[cu][1]),"+v"(B[cu][2]),"+v"(B[cu][3]),"+v"(B[cu][4]),"+v"(B[cu][5]),"+v"(B[cu][6]),"+v"(B[cu][7])::"memory");
      int g=0;
      #pragma unroll
      for(int qq=0;qq<8;qq++){
        u32x4 af=cat(A[cu][qq][0],A[cu][qq][1]);
        #pragma unroll
        for(int mm=0;mm+qq<8;mm++){
          MF16(acc[mm+qq],af,B[cu][mm]);
          if(pp<7 && g<24){ const int i=g/3, z=g%3;
            if(z==0) RD2(A[nx][i][0],aA[i>>1],16*(pp+1)+128*(i&1),16*(pp+1)+128*(i&1)+1);
            else if(z==1) RD2(A[nx][i][1],aA[i>>1],16*(pp+1)+128*(i&1)+2,16*(pp+1)+128*(i&1)+3);
            else RDB(nx,pp+1,i); }
          g++;
        } }
    }
    u32x2 uu[8];
    #pragma unroll
    for(int n=0;n<8;n++) uu[n]=ldsr<u32x2>(cb+CH_U+512+512*n+32*r+16*(G>>1)+8*(1-(G&1)));
    asm volatile("s_nop 7\n\ts_nop 7":"+v"(acc[0]),"+v"(acc[1]),"+v"(acc[2]),"+v"(acc[3]),"+v"(acc[4]),"+v"(acc[5]),"+v"(acc[6]),"+v"(acc[7]));
    #pragma unroll
    for(int n=0;n<8;n++){ f32x4 y=acc[n];
      ldsw<u32x2>(cb+CH_C0+512*n+32*r+8*G,(u32x2){cvtpk(y[0]+bfhi(uu[n].y)*Dv,y[1]+bflo(uu[n].y)*Dv),cvtpk(y[2]+bfhi(uu[n].x)*Dv,y[3]+bflo(uu[n].x)*Dv)}); }
    BAR_L();
  }
  rsrc_t ry=mkrs(ws+OFF_YX,S_*D_*2);
  #pragma unroll
  for(int z=0;z<2;z++){ const int t=tid+512*z;
    unsigned y0=ldsr<unsigned>(CH_C0+t*4),y1=ldsr<unsigned>(CH_STR+CH_C0+t*4),y2=ldsr<unsigned>(2*CH_STR+CH_C0+t*4),y3=ldsr<unsigned>(3*CH_STR+CH_C0+t*4);
    bs128<SC1>((u32x4){__builtin_amdgcn_perm(y1,y0,0x05040100),__builtin_amdgcn_perm(y3,y2,0x05040100),__builtin_amdgcn_perm(y1,y0,0x07060302),__builtin_amdgcn_perm(y3,y2,0x07060302)},
               ry,((t>>3)*256+b)*128+(t&7)*16,0); }
}

DI void unpack8(u32x4 v,float* f){ f[0]=bflo(v.x); f[1]=bfhi(v.x); f[2]=bflo(v.y); f[3]=bfhi(v.y); f[4]=bflo(v.z); f[5]=bfhi(v.z); f[6]=bflo(v.w); f[7]=bfhi(v.w); }
DI u32x4 pack8(const float* f){ return (u32x4){cvtpk(f[0],f[1]),cvtpk(f[2],f[3]),cvtpk(f[4],f[5]),cvtpk(f[6],f[7])}; }
__global__ __launch_bounds__(256) void k_p2(const u16* q,char* ws){
  const int tid=threadIdx.x, w=__builtin_amdgcn_readfirstlane(tid>>6), lane=tid&63, b=blockIdx.x;
  const int r=lane&7,g=lane>>3, row=b*8+r;
  rsrc_t rq=mkrs(q,S_*D_*2), ry=mkrs(ws+OFF_YX,S_*D_*2), rm=mkrs(ws+OFF_MOD,12288);
  u32x4 qv[4], md[5][4]; u32x2 yv[4][2];
  const int ub=w*8+g;
  #pragma unroll
  for(int u=0;u<4;u++) qv[u]=bl128<NT>(rq,row*2048+(u*32+ub)*16,0);
  #pragma unroll
  for(int u=0;u<4;u++){
    #pragma unroll
    for(int z=0;z<2;z++) yv[u][z]=bl64<SC1>(ry,((b>>1)*256+2*(u*32+ub)+z)*128+(b&1)*64+r*8,0); }
  #pragma unroll
  for(int j=0;j<5;j++)
    #pragma unroll
    for(int u=0;u<4;u++) md[j][u]=bl128<SC1>(rm,j*2048+(u*32+ub)*16,0);
  float x[32];
  #pragma unroll
  for(int u=0;u<4;u++) unpack8(qv[u],x+8*u);
  auto rowsum=[&](float v,int slot){ v=xsum8_16_32(v); if(lane<8) ldsw<float>(slot*128+lane*16+w*4,v); BAR_L(); f32x4 t=ldsr<f32x4>(slot*128+r*16); return (t[0]+t[1])+(t[2]+t[3]); };
  float s=0; for(int i=0;i<32;i++) s+=x[i];
  float mu=rowsum(s,0)*(1.f/1024); s=0; for(int i=0;i<32;i++){ float d=x[i]-mu; s+=d*d; }
  float rstd=__builtin_amdgcn_rsqf(rowsum(s,1)*(1.f/1024)+1e-5f);
  s=0;
  rsrc_t rx=mkrs(ws+OFF_X1,S_*D_*2), rh=mkrs(ws+OFF_H2,S_*D_*2);
  const unsigned tb=((row>>7)*32*8+((row>>4)&7))*1024+(row&15)*16;
  #pragma unroll
  for(int u=0;u<4;u++){ float sa[8],ca[8],ga[8],y[8]; unpack8(md[0][u],sa); unpack8(md[1][u],ca); unpack8(md[2][u],ga); unpack8(cat(yv[u][0],yv[u][1]),y);
    #pragma unroll
    for(int e=0;e<8;e++){ float h=(x[8*u+e]-mu)*rstd*rbf(1.f+ca[e])+sa[e]; x[8*u+e]+=ga[e]*h*y[e]; }
    u32x4 o=pack8(x+8*u); unpack8(o,x+8*u);
    const int un=u*32+ub; bs128<SC1>(o,rx,tb+(un>>2)*8192+(un&3)*256,0);
    #pragma unroll
    for(int e=0;e<8;e++) s+=x[8*u+e]; }
  mu=rowsum(s,2)*(1.f/1024); s=0; for(int i=0;i<32;i++){ float d=x[i]-mu; s+=d*d; }
  rstd=__builtin_amdgcn_rsqf(rowsum(s,3)*(1.f/1024)+1e-5f);
  #pragma unroll
  for(int u=0;u<4;u++){ float sb[8],cb[8],h[8]; unpack8(md[3][u],sb); unpack8(md[4][u],cb);
    #pragma unroll
    for(int e=0;e<8;e++) h[e]=(x[8*u+e]-mu)*rstd*rbf(1.f+cb[e])+sb[e];
    const int un=u*32+ub; bs128<SC1>(pack8(h),rh,tb+(un>>2)*8192+(un&3)*256,0); }
}

constexpr int G1_SLOT=22528, G1_R=7, G1_NT=32;
__global__ __launch_bounds__(512,2) void k_g1(const u16* w1,char* ws){
  const int tid=threadIdx.x, wid=__builtin_amdgcn_readfirstlane(tid>>6), lane=tid&63, b=blockIdx.x;
  const int xcd=b&7, idx=b>>3, li=idx&7, lj=idx>>3;
  const int pm=(xcd>>2)*8+li, pn=(xcd&3)*4+lj;
  if (wid>=4){
    if (wid<6){
      const int rot=(li+2*lj)&7, xrot=(((xcd&3)+2*(xcd>>2))&3)*8;
      const int w=wid-4; rsrc_t ra=mkrs(ws+OFF_H2,S_*D_*2), rb=mkrs(w1,D_*F_*2);
      const unsigned va=pm*32*8192+w*4096+lane*16; unsigned vb[7];
      #pragma unroll
      for(int i=0;i<7;i++){ int e=(7*w+i)*64+lane; int row=e/28, c=e-row*28; c^=((row>>3)&1)<<1; vb[i]=row*(F_*2)+c*16+pn*448; }
      auto issue=[&](int t,int slot){ const int kt=((t&~7)+((t+rot)&7)+xrot)&31; const unsigned l=slot*G1_SLOT;
        #pragma unroll
        for(int i=0;i<4;i++) bl_lds<SC1>(ra,l+w*4096+i*1024,va+i*1024,kt*8192);
        #pragma unroll
        for(int i=0;i<7;i++) bl_lds<0>(rb,l+8192+(7*w+i)*1024,vb[i],kt*32*F_*2); };
      #pragma unroll
      for(int t=0;t<5;t++) issue(t,t);
      BAR_V(44); issue(5,5); BAR_V(44);
      int slot=6;
      for(int s=1;s<G1_NT-5;s++){ issue(s+5,slot); slot=slot==G1_R-1?0:slot+1; BAR_V(44); }
      BAR_V(33); BAR_V(22); BAR_V(11); BAR_V(0);
    } else { for(int s=0;s<G1_NT;s++) BAR(); }
    return;
  }
  const int wm=wid>>1, wn=wid&1, G=lane>>4, q=(lane>>2)&3, p=lane&3, sw=(G&1)*32;
  const unsigned kp=8192+(8*G+q)*448+8*p;
  const unsigned aA=wm*4096+lane*16, aE=kp+256*wn+sw, aO=kp+256*wn-sw, aS=kp+(wn? 224-sw : 192+sw);
  f32x4 acc[7][4];
  #pragma unroll
  for(int j=0;j<7;j++)
    #pragma unroll
    for(int i=0;i<4;i++){ acc[j][i]=(f32x4){0,0,0,0}; asm volatile("":"+v"(acc[j][i])); }
  u32x4 X[2][4]; u32x2 W[2][7][2];
  auto rd=[&](int bf,int rr,unsigned a,unsigned e,unsigned o,unsigned sgl){
    const int z= rr==0?0 : rr<3? 3+rr : rr<6? rr-2 : rr;
    if(z<4) RD128(X[bf][z],a,z*1024);
    else { const int j=(z-4)>>1,t=(z-4)&1; if(j<6) RDTR(W[bf][j][t],(j&1)?o:e,32*j+1792*t); else RDTR(W[bf][6][t],sgl,1792*t); } };
  auto step=[&](int cu,bool more,unsigned so){
    const unsigned a=aA+so,e=aE+so,o=aO+so,sgl=aS+so;
    #pragma unroll
    for(int g=0;g<28;g++){ const int j=g>>2,i=g&3;
      MF16(acc[j][i],cat(W[cu][j][0],W[cu][j][1]),X[cu][i]);
      if(more && g<18) rd(cu^1,g,a,e,o,sgl); }
    LGKM(0); };
  BAR();
  #pragma unroll
  for(int i=0;i<18;i++) rd(0,i,aA,aE,aO,aS);
  LGKM(0);
  unsigned so=G1_SLOT;
  for(int s=0;s<G1_NT-2;s+=2){
    BAR(); step(0,true,so); so+=G1_SLOT; if(so==G1_R*G1_SLOT) so=0;
    BAR(); step(1,true,so); so+=G1_SLOT; if(so==G1_R*G1_SLOT) so=0; }
  BAR(); step(0,true,so); step(1,false,0);
  MFMA_DRAIN();
  rsrc_t rs=mkrs(ws+OFF_HT,S_*F_*2);
  const unsigned hb=((pm*112+pn*7)*8+wm*4)*1024+lane*16;
  #pragma unroll
  for(int i=0;i<4;i++){ unsigned o[14];
    #pragma unroll
    for(int j=0;j<7;j++){ asm volatile("":"+v"(acc[j][i])); f32x4 v=acc[j][i]; o[2*j]=cvtpk(silu_f(v[0]),silu_f(v[1])); o[2*j+1]=cvtpk(silu_f(v[2]),silu_f(v[3])); }
    #pragma unroll
    for(int u=0;u<3;u++) bs128<SC1>((u32x4){o[4*u],o[4*u+1],o[4*u+2],o[4*u+3]},rs,hb+(u+4*wn)*8192+i*1024,0);
    bs64<SC1>((u32x2){o[12],o[13]},rs,hb+3*8192+i*1024+wn*8,0); }
}

constexpr int G2_NG=28;
__global__ __launch_bounds__(512,2) void k_g2(const u16* w2,char* ws,u16* out){
  const int tid=threadIdx.x, wid=__builtin_amdgcn_readfirstlane(tid>>6), lane=tid&63, b=blockIdx.x;
  const int xcd=b&7, idx=b>>3, li=idx&7, lj=idx>>3;
  const int pm=(xcd>>2)*8+li, pj=(xcd&3)*4+lj, pn=pj&7, kh=pj>>3;
  if (wid>=4){
    const int w=wid-4, rot=(li+2*lj)&7, xrot=((xcd&1)+2*(xcd>>2))*14;
    rsrc_t ra=mkrs(ws+OFF_HT,S_*F_*2), rb=mkrs(w2,D_*F_*2);
    const unsigned va=(pm*112+kh*56)*8192+w*2048+lane*16; unsigned vb[2];
    #pragma unroll
    for(int i=0;i<2;i++){ int e=(2*w+i)*64+lane; int row=e>>4,pos=e&15; int c=(((pos>>1)^(row&7))<<1)|(pos&1); vb[i]=(kh*1792+row)*2048+pn*256+c*16; }
    auto issue=[&](int gI){
      #pragma unroll
      for(int z=0;z<2;z++){ const int t=2*gI+z; int kt=(t&~7)+((t+rot)&7)+xrot; if(kt>=56) kt-=56; const unsigned l=(gI&3)*32768+z*16384;
        #pragma unroll
        for(int i=0;i<2;i++) bl_lds<SC1>(ra,l+w*2048+i*1024,va+i*1024,kt*8192);
        #pragma unroll
        for(int i=0;i<2;i++) bl_lds<0>(rb,l+8192+(2*w+i)*1024,vb[i],kt*65536); } };
    issue(0); issue(1); issue(2);
    BAR_V(16);
    BAR_V(8);
    for(int s=1;s<G2_NG-2;s++){ issue(s+2); BAR_V(8); }
    BAR_V(0);
    BAR(); BAR();
    return;
  }
  const int wm=wid>>1, wn=wid&1, G=lane>>4, q=(lane>>2)&3, p=lane&3, kk=(4*G+q);
  const unsigned aA=wm*4096+lane*16; unsigned aW[4];
  #pragma unroll
  for(int j=0;j<4;j++){ const int jt=(((j>>1)^kh)*4+2*wn+(j&1)); aW[j]=8192+kk*256+((jt^(kk&7))*32)+8*p; }
  f32x4 acc[4][4];
  #pragma unroll
  for(int j=0;j<4;j++)
    #pragma unroll
    for(int i=0;i<4;i++){ acc[j][i]=(f32x4){0,0,0,0}; asm volatile("":"+v"(acc[j][i])); }
  u32x4 X[2][4]; u32x2 W[2][4][2];
  auto rd=[&](int bf,int rr,unsigned a,const unsigned* e,int off){
    const int z= rr==0?0 : rr<3? 3+rr : rr<6? rr-2 : rr;
    if(z<4) RD128(X[bf][z],a,off+z*1024); else { const int j=(z-4)>>1,t=(z-4)&1; RDTR(W[bf][j][t],e[j],off+4096*t); } };
  auto unit=[&](int cu,bool more,unsigned a,const unsigned* e,int off){
    #pragma unroll
    for(int g=0;g<16;g++){ const int j=g>>2,i=g&3;
      MF16(acc[j][i],cat(W[cu][j][0],W[cu][j][1]),X[cu][i]);
      if(more && g<12) rd(cu^1,g,a,e,off); }
    LGKM(0); };
  BAR();
  #pragma unroll
  for(int i=0;i<12;i++) rd(0,i,aA,aW,0);
  LGKM(0);
  for(int s=0;s<G2_NG-1;s++){
    const unsigned so=(s&3)*32768, sn=((s+1)&3)*32768;
    { const unsigned e[4]={aW[0]+so,aW[1]+so,aW[2]+so,aW[3]+so}; unit(0,true,aA+so,e,16384); }
    BAR();
    { const unsigned e[4]={aW[0]+sn,aW[1]+sn,aW[2]+sn,aW[3]+sn}; unit(1,true,aA+sn,e,0); } }
  { const unsigned so=((G2_NG-1)&3)*32768; const unsigned e[4]={aW[0]+so,aW[1]+so,aW[2]+so,aW[3]+so}; unit(0,true,aA+so,e,16384); unit(1,false,0,e,0); }
  MFMA_DRAIN();
  rsrc_t rx=mkrs(ws+OFF_XCH,8u<<20);
  const unsigned me=(pm*8+pn)*2+kh;
  #pragma unroll
  for(int j=2;j<4;j++)
    #pragma unroll
    for(int i=0;i<4;i++){ asm volatile("":"+v"(acc[j][i])); bs128<SC1>(__builtin_bit_cast(u32x4,acc[j][i]),rx,me*32768+wid*8192+((j-2)*4+i)*1024+lane*16,0); }
  rsrc_t r1=mkrs(ws+OFF_X1,S_*D_*2), rm=mkrs(ws+OFF_MOD,12288);
  const int c0=pn*128+kh*64+wn*32; u32x2 xv[2][4],gbv[2];
  #pragma unroll
  for(int j=0;j<2;j++){ gbv[j]=bl64<SC1>(rm,(5*1024+c0+16*j+4*G)*2,0);
    #pragma unroll
    for(int i=0;i<4;i++) xv[j][i]=bl64<SC1>(r1,((pm*32+(c0>>5))*8+wm*4+i)*1024+(2*j+(G>>1))*256+(lane&15)*16+(G&1)*8,0); }
  BAR_V(0);
  GAS unsigned* fl=(GAS unsigned*)(ws+OFF_FLAG);
  if(wid==0){
    if(lane==0) __hip_atomic_store(fl+me*64,1u,__ATOMIC_RELAXED,__HIP_MEMORY_SCOPE_AGENT);
    const u64 t0=__builtin_amdgcn_s_memrealtime();
    while(__builtin_amdgcn_readfirstlane(__hip_atomic_load(fl+(me^1)*64,__ATOMIC_RELAXED,__HIP_MEMORY_SCOPE_AGENT))==0){
      if(__builtin_amdgcn_s_memrealtime()-t0>200000ull) break; __builtin_amdgcn_s_sleep(1); }
  }
  BAR();
  u32x4 pv[2][4];
  #pragma unroll
  for(int j=0;j<2;j++)
    #pragma unroll
    for(int i=0;i<4;i++) pv[j][i]=bl128<SC1>(rx,(me^1)*32768+wid*8192+(j*4+i)*1024+lane*16,0);
  rsrc_t ro=mkrs(out,S_*D_*2);
  #pragma unroll
  for(int i=0;i<4;i++){ u32x2 o[2];
    #pragma unroll
    for(int j=0;j<2;j++){ asm volatile("":"+v"(acc[j][i])); f32x4 v=acc[j][i]+__builtin_bit_cast(f32x4,pv[j][i]);
      o[j].x=cvtpk(bflo(xv[j][i].x)+bflo(gbv[j].x)*v[0],bfhi(xv[j][i].x)+bfhi(gbv[j].x)*v[1]);
      o[j].y=cvtpk(bflo(xv[j][i].y)+bflo(gbv[j].y)*v[2],bfhi(xv[j][i].y)+bfhi(gbv[j].y)*v[3]); }
    auto rx_=__builtin_amdgcn_permlane16_swap(o[0].x,o[1].x,false,false); auto ry_=__builtin_amdgcn_permlane16_swap(o[0].y,o[1].y,false,false);
    bs128<0>((u32x4){rx_[0],ry_[0],rx_[1],ry_[1]},ro,(pm*128+wm*64+i*16+(lane&15))*2048+(c0+16*(G&1)+8*(G>>1))*2,0); }
}

extern "C" void kernel_launch(void* const* d_in, const int* in_sizes, int n_in, void* d_out, int out_size, void* d_ws, size_t ws_size, hipStream_t stream){
  const u16* q=(const u16*)d_in[0]; const u16* k=(const u16*)d_in[1]; const u16* v=(const u16*)d_in[2];
  const u16* shk=(const u16*)d_in[3]; const u16* ssmk=(const u16*)d_in[4]; const u16* ssmD=(const u16*)d_in[5];
  const u16* w1=(const u16*)d_in[7]; const u16* w2=(const u16*)d_in[8]; const u16* c=(const u16*)d_in[9]; const u16* wm=(const u16*)d_in[10];
  char* ws=(char*)d_ws;
  hipFuncSetAttribute((const void*)k_g1,hipFuncAttributeMaxDynamicSharedMemorySize,G1_SLOT*G1_R);
  hipFuncSetAttribute((const void*)k_g2,hipFuncAttributeMaxDynamicSharedMemorySize,131072);
  k_p0<<<256,512,33792,stream>>>(k,v,shk,ws);
  k_p1<<<256,512,P1_LDS,stream>>>(ssmk,ssmD,c,wm,ws);
  k_p2<<<256,256,512,stream>>>(q,ws);
  k_g1<<<256,512,G1_SLOT*G1_R,stream>>>(w1,ws);
  k_g2<<<256,512,131072,stream>>>(w2,ws,(u16*)d_out);
}
```

```cpp
#include <hip/hip_runtime.h>
#include <hip/hip_bf16.h>
typedef unsigned short u16;
typedef unsigned long long u64;
typedef __bf16 bf16x2 __attribute__((ext_vector_type(2)));
typedef float f32x2 __attribute__((ext_vector_type(2)));
typedef float f32x4 __attribute__((ext_vector_type(4)));
typedef unsigned u32x2 __attribute__((ext_vector_type(2)));
typedef unsigned u32x4 __attribute__((ext_vector_type(4)));
#define LAS __attribute__((address_space(3)))
#define GAS __attribute__((address_space(1)))
#define DI __device__ __forceinline__
constexpr int S_=2048, D_=1024, F_=3584;
constexpr unsigned OFF_FLAG=0, OFF_MOD=131072, OFF_STAMP=262144, OFF_UT=1u<<20, OFF_YX=OFF_UT+(4u<<20), OFF_H2=OFF_YX+(4u<<20), OFF_X1=OFF_H2+(4u<<20),
                   OFF_XCH=OFF_X1+(4u<<20), OFF_HT=OFF_XCH+(8u<<20), WS_END=OFF_HT+S_*F_*2;
constexpr unsigned FL_E=0, FL_0=1024, FL_1=4096, FL_2=16384, FL_3=24576, FL_4=32768;
constexpr int SC1=16;
typedef __amdgpu_buffer_rsrc_t rsrc_t;
DI rsrc_t mkrs(const void* p, unsigned n){ return __builtin_amdgcn_make_buffer_rsrc((void*)p,0,n,0x00020000); }
DI unsigned cvtpk(float lo, float hi){ f32x2 v={lo,hi}; bf16x2 b=__builtin_convertvector(v,bf16x2); return __builtin_bit_cast(unsigned,b); }
DI float bflo(unsigned u){ return __uint_as_float(u<<16); }
DI float bfhi(unsigned u){ return __uint_as_float(u&0xffff0000u); }
DI float rbf(float x){ return bflo(cvtpk(x,x)); }
DI float silu_f(float x){ return x*__builtin_amdgcn_rcpf(1.f+__builtin_amdgcn_exp2f(x*-1.44269504089f)); }
template<int AUX> DI void bl_lds(rsrc_t rs, unsigned ldsoff, unsigned voff, unsigned soff){
  __builtin_amdgcn_raw_ptr_buffer_load_lds(rs,(LAS void*)(size_t)ldsoff,16,voff,soff,0,AUX); }
template<int AUX> DI u32x4 bl128(rsrc_t rs, unsigned voff, unsigned soff){ return __builtin_amdgcn_raw_buffer_load_b128(rs,voff,soff,AUX); }
template<int AUX> DI u32x2 bl64(rsrc_t rs, unsigned voff, unsigned soff){ return __builtin_amdgcn_raw_buffer_load_b64(rs,voff,soff,AUX); }
template<int AUX> DI void bs128(u32x4 v, rsrc_t rs, unsigned voff, unsigned soff){ __builtin_amdgcn_raw_buffer_store_b128(v,rs,voff,soff,AUX); }
template<int AUX> DI void bs64(u32x2 v, rsrc_t rs, unsigned voff, unsigned soff){ __builtin_amdgcn_raw_buffer_store_b64(v,rs,voff,soff,AUX); }
template<int AUX> DI void bs32(unsigned v, rsrc_t rs, unsigned voff, unsigned soff){ __builtin_amdgcn_raw_buffer_store_b32(v,rs,voff,soff,AUX); }
#define MF16(acc,a,b) asm volatile("v_mfma_f32_16x16x32_bf16 %0, %1, %2, %0" : "+v"(acc) : "v"(a), "v"(b))
#define RD128(d,a,o) asm volatile("ds_read_b128 %0, %1 offset:%2" : "=v"(d) : "v"(a), "i"(o))
#define RDTR(d,a,o)  asm volatile("ds_read_b64_tr_b16 %0, %1 offset:%2" : "=v"(d) : "v"(a), "i"(o))
#define RD2(d,a,o0,o1) asm volatile("ds_read2_b32 %0, %1 offset0:%2 offset1:%3" : "=v"(d) : "v"(a), "i"(o0), "i"(o1))
#define LGKM(n) asm volatile("s_waitcnt lgkmcnt(%0)"::"i"(n):"memory")
#define VMC(n) asm volatile("s_waitcnt vmcnt(%0)"::"i"(n):"memory")
#define BAR() asm volatile("s_barrier":::"memory")
#define BAR_L() asm volatile("s_waitcnt lgkmcnt(0)\n\ts_barrier":::"memory")
#define BAR_V(n) asm volatile("s_waitcnt vmcnt(%0)\n\ts_barrier"::"i"(n):"memory")
#define BAR_VL(n) asm volatile("s_waitcnt vmcnt(%0) lgkmcnt(0)\n\ts_barrier"::"i"(n):"memory")
#define MFMA_DRAIN() asm volatile("s_nop 7\n\ts_nop 7":::"memory")
#define PIN(x) asm volatile("":"+v"(x))
DI u32x4 cat(u32x2 a,u32x2 b){ return (u32x4){a.x,a.y,b.x,b.y}; }
template<class T> DI T ldsr(unsigned a){ return *(LAS T*)(size_t)a; }
template<class T> DI void ldsw(unsigned a, T v){ *(LAS T*)(size_t)a=v; }
DI float xsum8_16_32(float x){
  x+=__uint_as_float(__builtin_amdgcn_update_dpp(0u,__float_as_uint(x),0x128,0xf,0xf,false));
  { unsigned u=__float_as_uint(x); auto r=__builtin_amdgcn_permlane16_swap(u,u,false,false); const unsigned a=r[0],c=r[1]; x=__uint_as_float(a)+__uint_as_float(c); }
  { unsigned u=__float_as_uint(x); auto r=__builtin_amdgcn_permlane32_swap(u,u,false,false); const unsigned a=r[0],c=r[1]; x=__uint_as_float(a)+__uint_as_float(c); }
  return x; }
DI void unpack8(u32x4 v,float* f){ f[0]=bflo(v.x); f[1]=bfhi(v.x); f[2]=bflo(v.y); f[3]=bfhi(v.y); f[4]=bflo(v.z); f[5]=bfhi(v.z); f[6]=bflo(v.w); f[7]=bfhi(v.w); }
DI u32x4 pack8(const float* f){ return (u32x4){cvtpk(f[0],f[1]),cvtpk(f[2],f[3]),cvtpk(f[4],f[5]),cvtpk(f[6],f[7])}; }
DI void sig(char* ws, unsigned off, unsigned val){ __hip_atomic_store((GAS unsigned*)(ws+off),val,__ATOMIC_RELAXED,__HIP_MEMORY_SCOPE_AGENT); }
DI void poll1(const char* ws, unsigned voff, unsigned val){
  const u64 t0=__builtin_amdgcn_s_memrealtime();
  for(;;){ unsigned v; asm volatile("global_load_dword %0, %1, %2 sc1\n\ts_waitcnt vmcnt(0)":"=&v"(v):"v"(voff),"s"(ws):"memory");
    if(__all(v==val)) break; if(__builtin_amdgcn_s_memrealtime()-t0>2000000ull) break; __builtin_amdgcn_s_sleep(1); } }
DI void poll4(const char* ws, unsigned voff, unsigned val){
  const u64 t0=__builtin_amdgcn_s_memrealtime();
  for(;;){ u32x4 v; asm volatile("global_load_dwordx4 %0, %1, %2 sc1\n\ts_waitcnt vmcnt(0)":"=&v"(v):"v"(voff),"s"(ws):"memory");
    if(__all(v.x==val && v.y==val && v.z==val && v.w==val)) break; if(__builtin_amdgcn_s_memrealtime()-t0>2000000ull) break; __builtin_amdgcn_s_sleep(1); } }
#define STAMP(i)
#define STAMPH(i)

constexpr int CH_U=0, CH_C0=4608, CH_C1=CH_C0+4160, CH_STR=CH_C1+4160;
constexpr int L_SC=4*CH_STR, L_RED=L_SC+4096, L_P0=57344, L_ST=L_P0+33792;
constexpr int G1_SLOT=22528, G1_R=7, G1_NT=32, G2_NG=28, LDS_BYTES=163840;

__global__ __launch_bounds__(512,2) void k_all(const u16* q,const u16* k,const u16* v,const u16* shk,const u16* ssmk,const u16* ssmD,
                                               const u16* w1,const u16* w2,const u16* cvec,const u16* wmod,char* ws,u16* out){
  const int tid=threadIdx.x, wid=__builtin_amdgcn_readfirstlane(tid>>6), lane=tid&63, b=blockIdx.x;
  const int xcd=b&7, idx=b>>3, li=idx&7, lj=idx>>3;
  const int pm=(xcd>>2)*8+li, pj=(xcd&3)*4+lj, pn=pj&7, kh=pj>>3;
  const int cg=xcd*32+idx;
  const int w=wid&3, ch=cg*4+w; const unsigned cb=w*CH_STR;
  STAMP(0);
  { const int s0=idx*64, pos=lane&15, lr=lane>>4;
    rsrc_t rk=mkrs(k,S_*D_*2), rv=mkrs(v,S_*D_*2);
    #pragma unroll
    for(int z=0;z<3;z++){ const int i=wid+8*z; if(i<17){ int rho=4*i+lr; int f=(rho&3)|((rho>>3&1)<<2); int c=(((pos>>1)^f)<<1)|(pos&1);
        bl_lds<0>(rk,L_P0+i*1024,(s0-4+rho)*2048+xcd*256+c*16,0);
        if(z<2) bl_lds<0>(rv,L_P0+17408+i*1024,(s0+rho)*2048+xcd*256+c*16,0); } }
  }
  #define TP_LOAD() u32x2 tp; { const unsigned o=(xcd*128+wid*16+(lane&15))*8; asm volatile("global_load_dwordx2 %0, %1, %2":"=v"(tp):"v"(o),"s"(shk):"memory"); }
  auto p0_compute=[&](u32x2 tp){
    const int s0=idx*64, pos=lane&15, lr=lane>>4, chp=xcd*128+wid*16+pos;
    const float w0=bflo(tp.x),w1_=bfhi(tp.x),w2_=bflo(tp.y),w3=bfhi(tp.y);
    const int qq=pos>>2,p=pos&3,par=(lr&1)<<2;
    const unsigned a0=L_P0+(8*lr+qq)*256+((wid^(qq|par))*32)+p*8, a1=L_P0+(8*lr+qq)*256+((wid^(qq|(par^4)))*32)+p*8;
    u32x2 kk[2][3],vv[2][2];
    #pragma unroll
    for(int z=0;z<2;z++){ RDTR(kk[z][0],a0,z*8192); RDTR(kk[z][1],a0,z*8192+1024); RDTR(kk[z][2],a1,z*8192+2048); RDTR(vv[z][0],a0,17408+z*8192); RDTR(vv[z][1],a0,17408+z*8192+1024); }
    asm volatile("s_waitcnt lgkmcnt(0)":"+v"(kk[0][0]),"+v"(kk[0][1]),"+v"(kk[0][2]),"+v"(kk[1][0]),"+v"(kk[1][1]),"+v"(kk[1][2]),"+v"(vv[0][0]),"+v"(vv[0][1]),"+v"(vv[1][0]),"+v"(vv[1][1])::"memory");
    rsrc_t ru=mkrs(ws+OFF_UT,S_*D_*2);
    #pragma unroll
    for(int z=0;z<2;z++){ float kf[12],vf[8],u[8];
      #pragma unroll
      for(int j=0;j<3;j++){ kf[4*j]=bflo(kk[z][j].x); kf[4*j+1]=bfhi(kk[z][j].x); kf[4*j+2]=bflo(kk[z][j].y); kf[4*j+3]=bfhi(kk[z][j].y); }
      #pragma unroll
      for(int j=0;j<2;j++){ vf[4*j]=bflo(vv[z][j].x); vf[4*j+1]=bfhi(vv[z][j].x); vf[4*j+2]=bflo(vv[z][j].y); vf[4*j+3]=bfhi(vv[z][j].y); }
      #pragma unroll
      for(int i=0;i<8;i++) u[i]=(kf[i+1]*w3+kf[i+2]*w2_+kf[i+3]*w1_+kf[i+4]*w0)*vf[i];
      bs128<SC1>((u32x4){cvtpk(u[7],u[6]),cvtpk(u[5],u[4]),cvtpk(u[3],u[2]),cvtpk(u[1],u[0])},ru,chp*4096+(s0+8*(lr+4*z))*2,0); } };
  auto y_store=[&](){ rsrc_t ry=mkrs(ws+OFF_YX,S_*D_*2);
    #pragma unroll
    for(int z=0;z<2;z++){ const int t=tid+512*z;
      unsigned y0=ldsr<unsigned>(CH_C0+t*4),y1=ldsr<unsigned>(CH_STR+CH_C0+t*4),y2=ldsr<unsigned>(2*CH_STR+CH_C0+t*4),y3=ldsr<unsigned>(3*CH_STR+CH_C0+t*4);
      bs128<SC1>((u32x4){__builtin_amdgcn_perm(y1,y0,0x05040100),__builtin_amdgcn_perm(y3,y2,0x05040100),__builtin_amdgcn_perm(y1,y0,0x07060302),__builtin_amdgcn_perm(y3,y2,0x07060302)},
                 ry,((t>>3)*256+cg)*128+(t&7)*16,0); } };

  if (wid>=4){
    TP_LOAD();
    u32x4 hv[4]; unsigned hn[4]; u32x2 cv;
    { const unsigned o=ch*4096+lane*16; rsrc_t rh=mkrs(ssmk,S_*D_*2);
      #pragma unroll
      for(int i=0;i<4;i++){ asm volatile("buffer_load_dwordx4 %0, %1, %2, 0 offen offset:%3":"=v"(hv[i]):"v"(o),"s"(rh),"i"(i*1024):"memory");
                            asm volatile("buffer_load_dword %0, %1, %2, 0 offen offset:%3":"=v"(hn[i]):"v"(o),"s"(rh),"i"(i*1024+16):"memory"); }
      const unsigned oc=(w*256+lane*4)*2; asm volatile("global_load_dwordx2 %0, %1, %2":"=v"(cv):"v"(oc),"s"(cvec):"memory"); }
    asm volatile("s_waitcnt vmcnt(9)\n\ts_barrier":"+v"(tp)::"memory");
    p0_compute(tp);
    asm volatile("s_waitcnt vmcnt(2)":"+v"(hv[0]),"+v"(hv[1]),"+v"(hv[2]),"+v"(hv[3]),"+v"(hn[0]),"+v"(hn[1]),"+v"(hn[2]),"+v"(hn[3]),"+v"(cv)::"memory");
    #pragma unroll
    for(int i=0;i<4;i++){ const unsigned a=cb+32+i*1024+lane*16;
      ldsw<u32x4>(a+CH_C0,hv[i]);
      ldsw<u32x4>(a+CH_C1,(u32x4){__builtin_amdgcn_alignbit(hv[i].y,hv[i].x,16),__builtin_amdgcn_alignbit(hv[i].z,hv[i].y,16),__builtin_amdgcn_alignbit(hv[i].w,hv[i].z,16),__builtin_amdgcn_alignbit(hn[i],hv[i].w,16)}); }
    { unsigned h0=__builtin_amdgcn_readfirstlane(hv[0].x); if(lane<8){ ldsw<unsigned>(cb+CH_C0+lane*4,0u); ldsw<unsigned>(cb+CH_C1+lane*4, lane==7? h0<<16 : 0u); } }
    ldsw<u32x2>(L_SC+w*512+lane*8,(u32x2){cvtpk(silu_f(bflo(cv.x)),silu_f(bfhi(cv.x))),cvtpk(silu_f(bflo(cv.y)),silu_f(bfhi(cv.y)))});
    BAR_VL(0);
    const bool gv=b<96;
    {
      rsrc_t rw=mkrs(wmod,gv? 1024*6144*2 : 0);
      const int rr=lane>>3, g0=(rr>>1)&1; const unsigned lb=L_P0+w*8192;
      const unsigned vo0=(w*256+rr)*12288+b*128+((((lane>>1)&3)^g0)*2+(lane&1))*16, vo1=vo0^64;
      auto issue=[&](int st){
        #pragma unroll
        for(int i=0;i<4;i++) bl_lds<0>(rw,lb+(st&1)*4096+i*1024,(i&1)? vo1:vo0,(st*32+i*8)*12288); };
      issue(0); issue(1);
      BAR();
      if(gv){
        const int G=lane>>4,q4=(lane>>2)&3,gg=((q4>>1)&1)|((G&1)<<1);
        unsigned aw[4];
        #pragma unroll
        for(int j=0;j<4;j++) aw[j]=lb+(8*G+q4)*128+((j^gg)*32)+(lane&3)*8;
        unsigned as=L_SC+w*512+G*16; f32x4 ac[4];
        #pragma unroll
        for(int j=0;j<4;j++){ ac[j]=(f32x4){0,0,0,0}; PIN(ac[j]); }
        #pragma unroll 1
        for(int st=0;st<8;st+=2){
          #pragma unroll
          for(int z=0;z<2;z++){ u32x4 sv; u32x2 t[4][2];
            if(st+z<7) VMC(4); else VMC(0);
            RD128(sv,as,z*64);
            #pragma unroll
            for(int j=0;j<4;j++){ RDTR(t[j][0],aw[j],z*4096); RDTR(t[j][1],aw[j],z*4096+512); }
            LGKM(0);
            if(st<6) issue(st+z+2);
            #pragma unroll
            for(int j=0;j<4;j++) MF16(ac[j],sv,cat(t[j][0],t[j][1])); }
          as+=128; }
        MFMA_DRAIN();
        #pragma unroll
        for(int j=0;j<4;j++){ PIN(ac[j]); if(lane<16) ldsw<float>(L_RED+w*256+j*64+lane*4,ac[j][0]); }
      } }
    STAMPH(16);
    BAR_L();
    if(gv && wid==4 && lane<32){ f32x2 s=ldsr<f32x2>(L_RED+lane*8)+ldsr<f32x2>(L_RED+256+lane*8)+ldsr<f32x2>(L_RED+512+lane*8)+ldsr<f32x2>(L_RED+768+lane*8);
      bs32<SC1>(cvtpk(s.x,s.y),mkrs(ws+OFF_MOD,12288),b*128+lane*4,0); }
    y_store();
    BAR_V(0);
    BAR();
    BAR();
    BAR();
    BAR();
    if (wid<6){
      const int rot=(li+2*lj)&7, xrot=(((xcd&3)+2*(xcd>>2))&3)*8;
      rsrc_t ra=mkrs(ws+OFF_H2,S_*D_*2), rb=mkrs(w1,D_*F_*2);
      const unsigned va=pm*32*8192+w*4096+lane*16; unsigned vb[7];
      #pragma unroll
      for(int i=0;i<7;i++){ int e=(7*w+i)*64+lane; int row=e/28, c=e-row*28; c^=((row>>3)&1)<<1; vb[i]=row*(F_*2)+c*16+pj*448; }
      auto issue=[&](int t,int slot){ const int kt=((t&~7)+((t+rot)&7)+xrot)&31; const unsigned l=slot*G1_SLOT;
        #pragma unroll
        for(int i=0;i<4;i++) bl_lds<SC1>(ra,l+w*4096+i*1024,va+i*1024,kt*8192);
        #pragma unroll
        for(int i=0;i<7;i++) bl_lds<0>(rb,l+8192+(7*w+i)*1024,vb[i],kt*32*F_*2); };
      #pragma unroll 1
      for(int t=0;t<5;t++) issue(t,t);
      BAR_V(44); issue(5,5); BAR_V(44);
      int slot=6;
      #pragma unroll 1
      for(int s=1;s<G1_NT-5;s++){ issue(s+5,slot); slot=slot==G1_R-1?0:slot+1; BAR_V(44); }
      BAR_V(33); BAR_V(22); BAR_V(11); BAR_V(0);
    } else {
      #pragma unroll 1
      for(int s=0;s<G1_NT;s++) BAR(); }
    BAR();
    BAR();
    { const int rot=(li+2*lj)&7, xrot=((xcd&1)+2*(xcd>>2))*14;
      rsrc_t ra=mkrs(ws+OFF_HT,S_*F_*2), rb=mkrs(w2,D_*F_*2);
      const unsigned va=(pm*112+kh*56)*8192+w*2048+lane*16; unsigned vb[2];
      #pragma unroll
      for(int i=0;i<2;i++){ int e=(2*w+i)*64+lane; int row=e>>4,pos=e&15; int c=(((pos>>1)^(row&7))<<1)|(pos&1); vb[i]=(kh*1792+row)*2048+pn*256+c*16; }
      auto issue=[&](int gI){
        #pragma unroll
        for(int z=0;z<2;z++){ const int t=2*gI+z; int kt=(t&~7)+((t+rot)&7)+xrot; if(kt>=56) kt-=56; const unsigned l=(gI&3)*32768+z*16384;
          #pragma unroll
          for(int i=0;i<2;i++) bl_lds<SC1>(ra,l+w*2048+i*1024,va+i*1024,kt*8192);
          #pragma unroll
          for(int i=0;i<2;i++) bl_lds<0>(rb,l+8192+(2*w+i)*1024,vb[i],kt*65536); } };
      #pragma unroll 1
      for(int g=0;g<3;g++) issue(g);
      BAR_V(16); BAR_V(8);
      for(int s=1;s<G2_NG-2;s++){ issue(s+2); BAR_V(8); }
      BAR_V(0); BAR(); BAR(); }
    return;
  }
  const int r8=lane&7, ub=w*8+(lane>>3), row=pm*128+pj*8+r8;
  TP_LOAD();
  u32x4 qv[4]; unsigned ep, dv;
  { const unsigned o=row*2048+ub*16;
    #pragma unroll
    for(int u=0;u<4;u++) asm volatile("global_load_dwordx4 %0, %1, %2 offset:%3":"=v"(qv[u]):"v"(o),"s"(q),"i"(u*512):"memory");
    const unsigned z=0; asm volatile("global_load_dword %0, %1, %2 sc1":"=v"(ep):"v"(z),"s"(ws):"memory");
    const unsigned od=ch*2; asm volatile("global_load_ushort %0, %1, %2":"=v"(dv):"v"(od),"s"(ssmD):"memory"); }
  if(lane<32) ldsw<u32x4>(cb+CH_U+lane*16,(u32x4){0,0,0,0});
  asm volatile("s_waitcnt vmcnt(6)\n\ts_barrier":"+v"(tp)::"memory");
  STAMP(1);
  p0_compute(tp);
  asm volatile("s_waitcnt vmcnt(2)":"+v"(qv[0]),"+v"(qv[1]),"+v"(qv[2]),"+v"(qv[3]),"+v"(ep),"+v"(dv)::"memory");
  const unsigned tag=__builtin_amdgcn_readfirstlane(ep)+1;
  auto stats_put=[&](const float* x,int slot){ float s=0,s2=0;
    #pragma unroll
    for(int i=0;i<32;i++){ s+=x[i]; s2+=x[i]*x[i]; }
    s=xsum8_16_32(s); s2=xsum8_16_32(s2); if(lane<8) ldsw<f32x2>(L_ST+slot*256+lane*32+w*8,(f32x2){s,s2}); };
  auto stats_get=[&](int slot,float& mu,float& rstd){ f32x4 a=ldsr<f32x4>(L_ST+slot*256+r8*32), c=ldsr<f32x4>(L_ST+slot*256+r8*32+16);
    mu=((a[0]+a[2])+(c[0]+c[2]))*(1.f/1024); const float var=fmaxf(((a[1]+a[3])+(c[1]+c[3]))*(1.f/1024)-mu*mu,0.f); rstd=__builtin_amdgcn_rsqf(var+1e-5f); };
  { float x[32];
    #pragma unroll
    for(int u=0;u<4;u++) unpack8(qv[u],x+8*u);
    stats_put(x,0); }
  BAR_VL(0);
  STAMP(2);
  if(wid==0){ if(lane==0) sig(ws,FL_0+xcd*256+idx*4,tag); poll1(ws,FL_0+xcd*256+(lane&31)*4,tag); }
  float mu1,rstd1; stats_get(0,mu1,rstd1); PIN(mu1); PIN(rstd1);
  BAR();
  STAMP(3);
  { rsrc_t ru=mkrs(ws+OFF_UT,S_*D_*2);
    #pragma unroll
    for(int i=0;i<4;i++) bl_lds<SC1>(ru,cb+CH_U+512+i*1024,ch*4096+i*1024+lane*16,0);
    const int r=lane&15,G=lane>>4;
    const unsigned aB=cb+CH_U+32+32*r+16*G;
    const unsigned aA0=cb+((r&1)? CH_C0+2*(r+25-8*G) : CH_C1+2*(r+24-8*G));
    unsigned aA[4]={aA0,aA0+1024,aA0+2048,aA0+3072};
    f32x4 acc[8];
    #pragma unroll
    for(int n=0;n<8;n++){ acc[n]=(f32x4){0,0,0,0}; PIN(acc[n]); }
    u32x2 A[2][8][2]; u32x4 B[2][8];
    #define RDB(bf,pp,mm) RD128(B[bf][mm],aB,512*(mm)-64*(pp)+448)
    #pragma unroll
    for(int i=0;i<8;i++){ RD2(A[0][i][0],aA[i>>1],128*(i&1),128*(i&1)+1); RD2(A[0][i][1],aA[i>>1],128*(i&1)+2,128*(i&1)+3); }
    VMC(0);
    STAMP(4);
    #pragma unroll
    for(int i=0;i<8;i++) RDB(0,0,i);
    #pragma unroll
    for(int pp=0;pp<8;pp++){
      const int cu=pp&1, nx=cu^1;
      LGKM(0);
      int g=0;
      #pragma unroll
      for(int qq=0;qq<8;qq++){
        #pragma unroll
        for(int mm=0;mm+qq<8;mm++){
          MF16(acc[mm+qq],cat(A[cu][qq][0],A[cu][qq][1]),B[cu][mm]);
          if(pp<7 && g<24){ const int i=g/3, z=g%3;
            if(z==0) RD2(A[nx][i][0],aA[i>>1],16*(pp+1)+128*(i&1),16*(pp+1)+128*(i&1)+1);
            else if(z==1) RD2(A[nx][i][1],aA[i>>1],16*(pp+1)+128*(i&1)+2,16*(pp+1)+128*(i&1)+3);
            else RDB(nx,pp+1,i); }
          g++;
        } }
    }
    u32x2 uu[8];
    #pragma unroll
    for(int n=0;n<8;n++) uu[n]=ldsr<u32x2>(cb+CH_U+512+512*n+32*r+16*(G>>1)+8*(1-(G&1)));
    MFMA_DRAIN();
    const float Dv=bflo(dv);
    #pragma unroll
    for(int n=0;n<8;n++){ PIN(acc[n]); f32x4 y=acc[n];
      ldsw<u32x2>(cb+CH_C0+512*n+32*r+8*G,(u32x2){cvtpk(y[0]+bfhi(uu[n].y)*Dv,y[1]+bflo(uu[n].y)*Dv),cvtpk(y[2]+bfhi(uu[n].x)*Dv,y[3]+bflo(uu[n].x)*Dv)}); }
  }
  STAMP(5);
  BAR_L();
  y_store();
  BAR_V(0);
  STAMP(6);
  if(wid==0){ if(lane<8) sig(ws,FL_1+lane*1024+b*4,tag); poll4(ws,FL_1+xcd*1024+lane*16,tag); if(b==0 && lane==0) sig(ws,FL_E,tag); }
  BAR();
  STAMP(7);
  { rsrc_t ry=mkrs(ws+OFF_YX,S_*D_*2), rm=mkrs(ws+OFF_MOD,12288);
    u32x4 md[5][4]; u32x2 yv[4][2];
    #pragma unroll
    for(int u=0;u<4;u++){
      #pragma unroll
      for(int z=0;z<2;z++) yv[u][z]=bl64<SC1>(ry,((row>>4)*256+2*ub)*128+(row&15)*8,(64*u+z)*128);
      #pragma unroll
      for(int j=0;j<3;j++) md[j][u]=bl128<SC1>(rm,ub*16,j*2048+u*512); }
    #pragma unroll
    for(int j=3;j<5;j++)
      #pragma unroll
      for(int u=0;u<4;u++) md[j][u]=bl128<SC1>(rm,ub*16,j*2048+u*512);
    rsrc_t rx=mkrs(ws+OFF_X1,S_*D_*2), rh=mkrs(ws+OFF_H2,S_*D_*2);
    const unsigned tb=(pm*32*8+((row>>4)&7))*1024+(row&15)*16+(ub>>2)*8192+(ub&3)*256;
    float x[32];
    #pragma unroll
    for(int u=0;u<4;u++){ float sa[8],ca[8],ga[8],y[8]; unpack8(qv[u],x+8*u); unpack8(md[0][u],sa); unpack8(md[1][u],ca); unpack8(md[2][u],ga); unpack8(cat(yv[u][0],yv[u][1]),y);
      #pragma unroll
      for(int e=0;e<8;e++){ float h=(x[8*u+e]-mu1)*rstd1*rbf(1.f+ca[e])+sa[e]; x[8*u+e]+=ga[e]*h*y[e]; }
      u32x4 o=pack8(x+8*u); unpack8(o,x+8*u);
      bs128<SC1>(o,rx,tb,u*65536); }
    stats_put(x,1);
    BAR_L();
    float mu,rstd; stats_get(1,mu,rstd);
    #pragma unroll
    for(int u=0;u<4;u++){ float sb[8],cb_[8],h[8]; unpack8(md[3][u],sb); unpack8(md[4][u],cb_);
      #pragma unroll
      for(int e=0;e<8;e++) h[e]=(x[8*u+e]-mu)*rstd*rbf(1.f+cb_[e])+sb[e];
      bs128<SC1>(pack8(h),rh,tb,u*65536); }
  }
  STAMP(8);
  BAR_V(0);
  STAMP(9);
  if(wid==0){ if(lane==0) sig(ws,FL_2+pm*256+pj*4,tag); poll1(ws,FL_2+pm*256+(lane&15)*4,tag); }
  BAR();
  STAMP(10);
  const int wm=wid>>1, wn=wid&1, G=lane>>4, q4=(lane>>2)&3, p4=lane&3;
  { const int sw=(G&1)*32; const unsigned kp=8192+(8*G+q4)*448+8*p4;
    const unsigned aA=wm*4096+lane*16, aE=kp+256*wn+sw, aO=kp+256*wn-sw, aS=kp+(wn? 224-sw : 192+sw);
    f32x4 acc[7][4];
    #pragma unroll
    for(int j=0;j<7;j++)
      #pragma unroll
      for(int i=0;i<4;i++){ acc[j][i]=(f32x4){0,0,0,0}; PIN(acc[j][i]); }
    u32x4 X[2][4]; u32x2 W[2][7][2];
    auto rd=[&](int bf,int rr,unsigned a,unsigned e,unsigned o,unsigned sgl){
      const int z= rr==0?0 : rr<3? 3+rr : rr<6? rr-2 : rr;
      if(z<4) RD128(X[bf][z],a,z*1024);
      else { const int j=(z-4)>>1,t=(z-4)&1; if(j<6) RDTR(W[bf][j][t],(j&1)?o:e,32*j+1792*t); else RDTR(W[bf][6][t],sgl,1792*t); } };
    auto step=[&](int cu,bool more,unsigned so){
      const unsigned a=aA+so,e=aE+so,o=aO+so,sgl=aS+so;
      #pragma unroll
      for(int g=0;g<28;g++){ const int j=g>>2,i=g&3;
        MF16(acc[j][i],cat(W[cu][j][0],W[cu][j][1]),X[cu][i]);
        if(more && g<18) rd(cu^1,g,a,e,o,sgl); }
      LGKM(0); };
    BAR();
    STAMP(11);
    #pragma unroll
    for(int i=0;i<18;i++) rd(0,i,aA,aE,aO,aS);
    LGKM(0);
    unsigned so=G1_SLOT;
    for(int s=0;s<G1_NT-2;s+=2){
      BAR(); step(0,true,so); so+=G1_SLOT; if(so==G1_R*G1_SLOT) so=0;
      BAR(); step(1,true,so); so+=G1_SLOT; if(so==G1_R*G1_SLOT) so=0; }
    BAR(); step(0,true,so); step(1,false,0);
    MFMA_DRAIN();
    STAMP(12);
    rsrc_t rs=mkrs(ws+OFF_HT,S_*F_*2);
    const unsigned hb=((pm*112+pj*7)*8+wm*4)*1024+lane*16;
    #pragma unroll
    for(int i=0;i<4;i++){ unsigned o[14];
      #pragma unroll
      for(int j=0;j<7;j++){ PIN(acc[j][i]); f32x4 v_=acc[j][i]; o[2*j]=cvtpk(silu_f(v_[0]),silu_f(v_[1])); o[2*j+1]=cvtpk(silu_f(v_[2]),silu_f(v_[3])); }
      #pragma unroll
      for(int u=0;u<3;u++) bs128<SC1>((u32x4){o[4*u],o[4*u+1],o[4*u+2],o[4*u+3]},rs,hb+(u+4*wn)*8192+i*1024,0);
      bs64<SC1>((u32x2){o[12],o[13]},rs,hb+3*8192+i*1024+wn*8,0); }
  }
  STAMP(13);
  BAR_V(0);
  STAMP(14);
  if(wid==0){ if(lane==0) sig(ws,FL_3+pm*256+pj*4,tag); poll1(ws,FL_3+pm*256+(kh*8+(lane&7))*4,tag); }
  BAR();
  STAMP(15);
  { const int kk=4*G+q4; const unsigned aA=wm*4096+lane*16; unsigned aW[4];
    #pragma unroll
    for(int j=0;j<4;j++){ const int jt=(((j>>1)^kh)*4+2*wn+(j&1)); aW[j]=8192+kk*256+((jt^(kk&7))*32)+8*p4; }
    f32x4 acc[4][4];
    #pragma unroll
    for(int j=0;j<4;j++)
      #pragma unroll
      for(int i=0;i<4;i++){ acc[j][i]=(f32x4){0,0,0,0}; PIN(acc[j][i]); }
    u32x4 X[2][4]; u32x2 W[2][4][2];
    auto rd=[&](int bf,int rr,unsigned a,const unsigned* e,int off){
      const int z= rr==0?0 : rr<3? 3+rr : rr<6? rr-2 : rr;
      if(z<4) RD128(X[bf][z],a,off+z*1024); else { const int j=(z-4)>>1,t=(z-4)&1; RDTR(W[bf][j][t],e[j],off+4096*t); } };
    auto unit=[&](int cu,bool more,unsigned a,const unsigned* e,int off){
      #pragma unroll
      for(int g=0;g<16;g++){ const int j=g>>2,i=g&3;
        MF16(acc[j][i],cat(W[cu][j][0],W[cu][j][1]),X[cu][i]);
        if(more && g<12) rd(cu^1,g,a,e,off); }
      LGKM(0); };
    BAR();
    STAMP(17);
    #pragma unroll
    for(int i=0;i<12;i++) rd(0,i,aA,aW,0);
    LGKM(0);
    for(int s=0;s<G2_NG-1;s++){
      const unsigned so=(s&3)*32768, sn=((s+1)&3)*32768;
      { const unsigned e[4]={aW[0]+so,aW[1]+so,aW[2]+so,aW[3]+so}; unit(0,true,aA+so,e,16384); }
      BAR();
      { const unsigned e[4]={aW[0]+sn,aW[1]+sn,aW[2]+sn,aW[3]+sn}; unit(1,true,aA+sn,e,0); } }
    { const unsigned so=((G2_NG-1)&3)*32768; const unsigned e[4]={aW[0]+so,aW[1]+so,aW[2]+so,aW[3]+so}; unit(0,true,aA+so,e,16384); unit(1,false,0,e,0); }
    MFMA_DRAIN();
    STAMP(18);
    rsrc_t rx=mkrs(ws+OFF_XCH,8u<<20);
    const unsigned me=(pm*8+pn)*2+kh;
    #pragma unroll
    for(int j=2;j<4;j++)
      #pragma unroll
      for(int i=0;i<4;i++){ PIN(acc[j][i]); bs128<SC1>(__builtin_bit_cast(u32x4,acc[j][i]),rx,me*32768+wid*8192+((j-2)*4+i)*1024+lane*16,0); }
    rsrc_t r1=mkrs(ws+OFF_X1,S_*D_*2), rm=mkrs(ws+OFF_MOD,12288);
    const int c0=pn*128+kh*64+wn*32; u32x2 xv[2][4],gbv[2];
    #pragma unroll
    for(int j=0;j<2;j++){ gbv[j]=bl64<SC1>(rm,(5*1024+c0+16*j+4*G)*2,0);
      #pragma unroll
      for(int i=0;i<4;i++) xv[j][i]=bl64<SC1>(r1,((pm*32+(c0>>5))*8+wm*4+i)*1024+(2*j+(G>>1))*256+(lane&15)*16+(G&1)*8,0); }
    BAR_V(0);
    STAMP(19);
    if(wid==0){ if(lane==0) sig(ws,FL_4+me*128,tag); poll1(ws,FL_4+(me^1)*128,tag); }
    BAR();
    STAMP(20);
    u32x4 pv[2][4];
    #pragma unroll
    for(int j=0;j<2;j++)
      #pragma unroll
      for(int i=0;i<4;i++) pv[j][i]=bl128<SC1>(rx,(me^1)*32768+wid*8192+(j*4+i)*1024+lane*16,0);
    rsrc_t ro=mkrs(out,S_*D_*2);
    #pragma unroll
    for(int i=0;i<4;i++){ u32x2 o[2];
      #pragma unroll
      for(int j=0;j<2;j++){ PIN(acc[j][i]); f32x4 v_=acc[j][i]+__builtin_bit_cast(f32x4,pv[j][i]);
        o[j].x=cvtpk(bflo(xv[j][i].x)+bflo(gbv[j].x)*v_[0],bfhi(xv[j][i].x)+bfhi(gbv[j].x)*v_[1]);
        o[j].y=cvtpk(bflo(xv[j][i].y)+bflo(gbv[j].y)*v_[2],bfhi(xv[j][i].y)+bfhi(gbv[j].y)*v_[3]); }
      auto sx=__builtin_amdgcn_permlane16_swap(o[0].x,o[1].x,false,false); auto sy=__builtin_amdgcn_permlane16_swap(o[0].y,o[1].y,false,false);
      bs128<SC1>((u32x4){sx[0],sy[0],sx[1],sy[1]},ro,(pm*128+wm*64+i*16+(lane&15))*2048+(c0+16*(G&1)+8*(G>>1))*2,0); }
    STAMP(21);
  }
}

extern "C" void kernel_launch(void* const* d_in, const int* in_sizes, int n_in, void* d_out, int out_size, void* d_ws, size_t ws_size, hipStream_t stream){
  (void)hipFuncSetAttribute((const void*)k_all,hipFuncAttributeMaxDynamicSharedMemorySize,LDS_BYTES);
  k_all<<<256,512,LDS_BYTES,stream>>>((const u16*)d_in[0],(const u16*)d_in[1],(const u16*)d_in[2],(const u16*)d_in[3],(const u16*)d_in[4],(const u16*)d_in[5],
                                      (const u16*)d_in[7],(const u16*)d_in[8],(const u16*)d_in[9],(const u16*)d_in[10],(char*)d_ws,(u16*)d_out);
}
```

```cpp
#include <hip/hip_runtime.h>
#include <hip/hip_bf16.h>
typedef unsigned short u16;
typedef unsigned long long u64;
typedef __bf16 bf16x2 __attribute__((ext_vector_type(2)));
typedef float f32x2 __attribute__((ext_vector_type(2)));
typedef float f32x4 __attribute__((ext_vector_type(4)));
typedef unsigned u32x2 __attribute__((ext_vector_type(2)));
typedef unsigned u32x4 __attribute__((ext_vector_type(4)));
#define LAS __attribute__((address_space(3)))
#define GAS __attribute__((address_space(1)))
#define DI __device__ __forceinline__
constexpr int S_=2048, D_=1024, F_=3584;
constexpr unsigned OFF_FLAG=0, OFF_MOD=327680, OFF_STAMP=393216, OFF_UT=1u<<20, OFF_YX=OFF_UT+(4u<<20), OFF_H2=OFF_YX+(4u<<20), OFF_X1=OFF_H2+(4u<<20),
                   OFF_XCH=OFF_X1+(4u<<20), OFF_HT=OFF_XCH+(8u<<20), WS_END=OFF_HT+S_*F_*2;
constexpr unsigned FL_E=0, FL_M=4096, FL_0=32768, FL_1=65536, FL_2=131072, FL_3=163840, FL_4=196608;
constexpr int SC1=16;
typedef __amdgpu_buffer_rsrc_t rsrc_t;
DI rsrc_t mkrs(const void* p, unsigned n){ return __builtin_amdgcn_make_buffer_rsrc((void*)p,0,n,0x00020000); }
DI unsigned cvtpk(float lo, float hi){ f32x2 v={lo,hi}; bf16x2 b=__builtin_convertvector(v,bf16x2); return __builtin_bit_cast(unsigned,b); }
DI float bflo(unsigned u){ return __uint_as_float(u<<16); }
DI float bfhi(unsigned u){ return __uint_as_float(u&0xffff0000u); }
DI float rbf(float x){ return bflo(cvtpk(x,x)); }
DI float silu_f(float x){ return x*__builtin_amdgcn_rcpf(1.f+__builtin_amdgcn_exp2f(x*-1.44269504089f)); }
template<int AUX> DI void bl_lds(rsrc_t rs, unsigned ldsoff, unsigned voff, unsigned soff){
  __builtin_amdgcn_raw_ptr_buffer_load_lds(rs,(LAS void*)(size_t)ldsoff,16,voff,soff,0,AUX); }
template<int AUX> DI u32x4 bl128(rsrc_t rs, unsigned voff, unsigned soff){ return __builtin_amdgcn_raw_buffer_load_b128(rs,voff,soff,AUX); }
template<int AUX> DI u32x2 bl64(rsrc_t rs, unsigned voff, unsigned soff){ return __builtin_amdgcn_raw_buffer_load_b64(rs,voff,soff,AUX); }
template<int AUX> DI void bs128(u32x4 v, rsrc_t rs, unsigned voff, unsigned off){ __builtin_amdgcn_raw_buffer_store_b128(v,rs,voff+off,0,AUX); }
template<int AUX> DI void bs64(u32x2 v, rsrc_t rs, unsigned voff, unsigned off){ __builtin_amdgcn_raw_buffer_store_b64(v,rs,voff+off,0,AUX); }
template<int AUX> DI void bs32(unsigned v, rsrc_t rs, unsigned voff, unsigned soff){ __builtin_amdgcn_raw_buffer_store_b32(v,rs,voff,soff,AUX); }
#define MF16(acc,a,b) asm volatile("v_mfma_f32_16x16x32_bf16 %0, %1, %2, %0" : "+v"(acc) : "v"(a), "v"(b))
#define RD128(d,a,o) asm volatile("ds_read_b128 %0, %1 offset:%2" : "=v"(d) : "v"(a), "i"(o))
#define RDTR(d,a,o)  asm volatile("ds_read_b64_tr_b16 %0, %1 offset:%2" : "=v"(d) : "v"(a), "i"(o))
#define RD2(d,a,o0,o1) asm volatile("ds_read2_b32 %0, %1 offset0:%2 offset1:%3" : "=v"(d) : "v"(a), "i"(o0), "i"(o1))
#define LGKM(n) asm volatile("s_waitcnt lgkmcnt(%0)"::"i"(n):"memory")
#define VMC(n) asm volatile("s_waitcnt vmcnt(%0)"::"i"(n):"memory")
#define BAR() asm volatile("s_barrier":::"memory")
#define BAR_L() asm volatile("s_waitcnt lgkmcnt(0)\n\ts_barrier":::"memory")
#define BAR_V(n) asm volatile("s_waitcnt vmcnt(%0)\n\ts_barrier"::"i"(n):"memory")
#define BAR_VL(n) asm volatile("s_waitcnt vmcnt(%0) lgkmcnt(0)\n\ts_barrier"::"i"(n):"memory")
#define MFMA_DRAIN() asm volatile("s_nop 7\n\ts_nop 7":::"memory")
#define PIN(x) asm volatile("":"+v"(x))
DI u32x4 cat(u32x2 a,u32x2 b){ return (u32x4){a.x,a.y,b.x,b.y}; }
template<class T> DI T ldsr(unsigned a){ return *(LAS T*)(size_t)a; }
template<class T> DI void ldsw(unsigned a, T v){ *(LAS T*)(size_t)a=v; }
DI float xsum8_16_32(float x){
  x+=__uint_as_float(__builtin_amdgcn_update_dpp(0u,__float_as_uint(x),0x128,0xf,0xf,false));
  { unsigned u=__float_as_uint(x); auto r=__builtin_amdgcn_permlane16_swap(u,u,false,false); const unsigned a=r[0],c=r[1]; x=__uint_as_float(a)+__uint_as_float(c); }
  { unsigned u=__float_as_uint(x); auto r=__builtin_amdgcn_permlane32_swap(u,u,false,false); const unsigned a=r[0],c=r[1]; x=__uint_as_float(a)+__uint_as_float(c); }
  return x; }
DI void unpack8(u32x4 v,float* f){ f[0]=bflo(v.x); f[1]=bfhi(v.x); f[2]=bflo(v.y); f[3]=bfhi(v.y); f[4]=bflo(v.z); f[5]=bfhi(v.z); f[6]=bflo(v.w); f[7]=bfhi(v.w); }
DI u32x4 pack8(const float* f){ return (u32x4){cvtpk(f[0],f[1]),cvtpk(f[2],f[3]),cvtpk(f[4],f[5]),cvtpk(f[6],f[7])}; }
DI void sig(char* ws, unsigned off, unsigned val){ __hip_atomic_store((GAS unsigned*)(ws+off),val,__ATOMIC_RELAXED,__HIP_MEMORY_SCOPE_AGENT); }
DI void poll1(const char* ws, unsigned voff, unsigned val){
  const u64 t0=__builtin_amdgcn_s_memrealtime();
  for(;;){ unsigned v; asm volatile("global_load_dword %0, %1, %2 sc1\n\ts_waitcnt vmcnt(0)":"=&v"(v):"v"(voff),"s"(ws):"memory");
    if(__all(v==val)) break; if(__builtin_amdgcn_s_memrealtime()-t0>2000000ull) break; __builtin_amdgcn_s_sleep(1); } }
DI void poll4(const char* ws, unsigned voff, unsigned val){
  const u64 t0=__builtin_amdgcn_s_memrealtime();
  for(;;){ u32x4 v; asm volatile("global_load_dwordx4 %0, %1, %2 sc1\n\ts_waitcnt vmcnt(0)":"=&v"(v):"v"(voff),"s"(ws):"memory");
    if(__all(v.x==val && v.y==val && v.z==val && v.w==val)) break; if(__builtin_amdgcn_s_memrealtime()-t0>2000000ull) break; __builtin_amdgcn_s_sleep(1); } }
#define STAMP(i)
#define STAMPH(i)

#define MOD_F32
#define GVMODE 2
#define NTA 2
#define TOUCH_AT 31
#define TOUCH_W 3
#define TOUCH_SLEEP 3
constexpr int CODE_LINES=6;

constexpr int CH_B=128, CH_U=0, CH_C0=4608, CH_C1=CH_C0+4160, CH_STR=CH_C1+4160;
constexpr int L_SC=CH_B+4*CH_STR, L_RED=L_SC+1024, L_P0=57344, L_GV=91136, L_M=137216, L_ST=157696, L_CNT=L_ST+512;
constexpr int G1_SLOT=22528, G1_R=7, G1_NT=32, LDS_BYTES=163840;
#define ALOAD(ins,d,o,p) asm volatile(ins " %0, %1, %2":"=v"(d):"v"(o),"s"(p):"memory")
#define TOUCH(d,o,p) asm volatile("global_load_dword %0, %1, %2":"+v"(d):"v"(o),"s"(p):"memory")
#define ALOADO(ins,d,o,p,i) asm volatile(ins " %0, %1, %2 offset:%3":"=v"(d):"v"(o),"s"(p),"i"(i):"memory")

__global__ __launch_bounds__(512,2) void k_all(const u16* q,const u16* k,const u16* v,const u16* shk,const u16* ssmk,const u16* ssmD,
                                               const u16* w1,const u16* w2,const u16* cvec,const u16* wmod,char* ws,u16* out){
  u64 pc0; asm volatile("s_getpc_b64 %0":"=s"(pc0));
  const int tid=threadIdx.x, wid=__builtin_amdgcn_readfirstlane(tid>>6), lane=tid&63, b=blockIdx.x;
  const int xcd=b&7, idx=b>>3, li=idx&7, lj=idx>>3;
  const int pm=(xcd>>2)*8+li, pj=(xcd&3)*4+lj, pn=pj&7, kh=pj>>3;
  const int cg=xcd*32+idx;
  const int w=wid&3, ch=cg*4+w; const unsigned cb=CH_B+w*CH_STR;
  STAMP(0);
  { const int s0=idx*64, pos=lane&15, lr=lane>>4;
    rsrc_t rk=mkrs(k,S_*D_*2), rv=mkrs(v,S_*D_*2);
    const int f=lr|((wid>>1&1)<<2), c=(((pos>>1)^f)<<1)|(pos&1);
    const unsigned vo=(s0+4*wid+lr)*2048+xcd*256+c*16;
    bl_lds<0>(rk,L_P0+wid*1024,vo-8192,0); bl_lds<0>(rv,L_P0+17408+wid*1024,vo,0);
    bl_lds<0>(rk,L_P0+wid*1024+8192,vo+57344,0); bl_lds<0>(rv,L_P0+17408+wid*1024+8192,vo,65536);
    if(wid==0) bl_lds<0>(rk,L_P0+16384,vo+57344,65536);
  }
  #define TP_LOAD() u32x2 tp; { const unsigned o=(xcd*128+wid*16+(lane&15))*8; ALOAD("global_load_dwordx2",tp,o,shk); }
  auto p0_compute=[&](u32x2 tp){
    const int s0=idx*64, pos=lane&15, lr=lane>>4, chp=xcd*128+wid*16+pos;
    const float w0=bflo(tp.x),w1_=bfhi(tp.x),w2_=bflo(tp.y),w3=bfhi(tp.y);
    const int qq=pos>>2,p=pos&3,par=(lr&1)<<2;
    const unsigned a0=L_P0+(8*lr+qq)*256+((wid^(qq|par))*32)+p*8, a1=L_P0+(8*lr+qq)*256+((wid^(qq|(par^4)))*32)+p*8;
    u32x2 kk[2][3],vv[2][2];
    #pragma unroll
    for(int z=0;z<2;z++){ RDTR(kk[z][0],a0,z*8192); RDTR(kk[z][1],a0,z*8192+1024); RDTR(kk[z][2],a1,z*8192+2048); RDTR(vv[z][0],a0,17408+z*8192); RDTR(vv[z][1],a0,17408+z*8192+1024); }
    asm volatile("s_waitcnt lgkmcnt(0)":"+v"(kk[0][0]),"+v"(kk[0][1]),"+v"(kk[0][2]),"+v"(kk[1][0]),"+v"(kk[1][1]),"+v"(kk[1][2]),"+v"(vv[0][0]),"+v"(vv[0][1]),"+v"(vv[1][0]),"+v"(vv[1][1])::"memory");
    rsrc_t ru=mkrs(ws+OFF_UT,S_*D_*2);
    #pragma unroll
    for(int z=0;z<2;z++){ float kf[12],vf[8],u[8];
      #pragma unroll
      for(int j=0;j<3;j++){ kf[4*j]=bflo(kk[z][j].x); kf[4*j+1]=bfhi(kk[z][j].x); kf[4*j+2]=bflo(kk[z][j].y); kf[4*j+3]=bfhi(kk[z][j].y); }
      #pragma unroll
      for(int j=0;j<2;j++){ vf[4*j]=bflo(vv[z][j].x); vf[4*j+1]=bfhi(vv[z][j].x); vf[4*j+2]=bflo(vv[z][j].y); vf[4*j+3]=bfhi(vv[z][j].y); }
      #pragma unroll
      for(int i=0;i<8;i++) u[i]=(kf[i+1]*w3+kf[i+2]*w2_+kf[i+3]*w1_+kf[i+4]*w0)*vf[i];
      bs128<SC1>((u32x4){cvtpk(u[7],u[6]),cvtpk(u[5],u[4]),cvtpk(u[3],u[2]),cvtpk(u[1],u[0])},ru,chp*4096+(s0+8*(lr+4*z))*2,0); } };
  auto y_store=[&](){ rsrc_t ry=mkrs(ws+OFF_YX,S_*D_*2);
    #pragma unroll
    for(int z=0;z<2;z++){ const int t=tid+512*z; const unsigned a=CH_B+CH_C0+t*4;
      unsigned y0=ldsr<unsigned>(a),y1=ldsr<unsigned>(a+CH_STR),y2=ldsr<unsigned>(a+2*CH_STR),y3=ldsr<unsigned>(a+3*CH_STR);
      bs128<SC1>((u32x4){__builtin_amdgcn_perm(y1,y0,0x05040100),__builtin_amdgcn_perm(y3,y2,0x05040100),__builtin_amdgcn_perm(y1,y0,0x07060302),__builtin_amdgcn_perm(y3,y2,0x07060302)},
                 ry,((t>>3)*256+cg)*128+(t&7)*16,0); } };

  if (wid>=4){
    __builtin_amdgcn_s_setprio(3);
    TP_LOAD();
    unsigned dummy=0, ep;
    { const u64 pc=pc0&~255ull; const unsigned o= wid==7? (idx*CODE_LINES+min(lane,CODE_LINES-1))*128 : 0; TOUCH(dummy,o,pc); }
    { const unsigned z=0; asm volatile("global_load_dword %0, %1, %2 sc1":"=v"(ep):"v"(z),"s"(ws):"memory"); }
    __builtin_amdgcn_s_sleep(6);
    u32x4 hv[4]; unsigned cv;
    const bool gv=b<192; const int gc= b<96? b : b-96, gr=(b<96? 0:512)+w*128;
    { const unsigned o=ch*4096+lane*16;
      #pragma unroll
      for(int i=0;i<4;i++) ALOADO("global_load_dwordx4",hv[i],o,ssmk,i*1024);
      const unsigned oc=(gr+lane*2)*2; ALOAD("global_load_dword",cv,oc,cvec); }
    asm volatile("s_waitcnt vmcnt(5)\n\ts_barrier":"+v"(tp),"+v"(dummy),"+v"(ep)::"memory");
    const unsigned tag=__builtin_amdgcn_readfirstlane(ep)+1;
    p0_compute(tp);
    asm volatile("s_waitcnt vmcnt(0)\n\ts_barrier":"+v"(hv[0]),"+v"(hv[1]),"+v"(hv[2]),"+v"(hv[3]),"+v"(cv)::"memory");
    rsrc_t rw=mkrs(wmod,1024*6144*2);
    const unsigned vo0=(gr+(lane>>3))*12288+gc*128+((((lane>>1)&3)^((lane>>4)&1))*2+(lane&1))*16, vo1=vo0^64;
    auto gv_issue=[&](int st,unsigned l){
      #pragma unroll
      for(int i=0;i<4;i++) bl_lds<NTA>(rw,l+i*1024,(i&1)? vo1:vo0,(st*32+i*8)*12288); };
    f32x4 ac[4];
    auto gv_step=[&](int st,unsigned l){
      const int G=lane>>4,q4=(lane>>2)&3,gg=((q4>>1)&1)|((G&1)<<1);
      const unsigned aw0=l+(8*G+q4)*128+(lane&3)*8, as=L_SC+w*256+G*16+st*64;
      u32x4 sv; u32x2 t[4][2];
      RD128(sv,as,0);
      #pragma unroll
      for(int j=0;j<4;j++){ const unsigned a=aw0+((j^gg)*32); RDTR(t[j][0],a,0); RDTR(t[j][1],a,512); }
      LGKM(0);
      #pragma unroll
      for(int j=0;j<4;j++) MF16(ac[j],sv,cat(t[j][0],t[j][1])); };
    #pragma unroll
    for(int i=0;i<4;i++){ const unsigned a=cb+32+i*1024+lane*16;
      unsigned nx=__builtin_amdgcn_update_dpp(0u,hv[i].x,0x130,0xf,0xf,true);
      if(i<3){ const unsigned f=__builtin_amdgcn_readfirstlane(hv[i<3?i+1:i].x); nx= lane==63? f: nx; }
      ldsw<u32x4>(a+CH_C0,hv[i]);
      ldsw<u32x4>(a+CH_C1,(u32x4){__builtin_amdgcn_alignbit(hv[i].y,hv[i].x,16),__builtin_amdgcn_alignbit(hv[i].z,hv[i].y,16),__builtin_amdgcn_alignbit(hv[i].w,hv[i].z,16),__builtin_amdgcn_alignbit(nx,hv[i].w,16)}); }
    { unsigned h0=__builtin_amdgcn_readfirstlane(hv[0].x); if(lane<8){ ldsw<unsigned>(cb+CH_C0+lane*4,0u); ldsw<unsigned>(cb+CH_C1+lane*4, lane==7? h0<<16 : 0u); } }
    ldsw<unsigned>(L_SC+w*256+lane*4,cvtpk(silu_f(bflo(cv)),silu_f(bfhi(cv))));
    #pragma unroll
    for(int j=0;j<4;j++){ ac[j]=(f32x4){0,0,0,0}; PIN(ac[j]); }
    BAR_L();
    if(gv){
      __builtin_amdgcn_s_sleep(TOUCH_SLEEP);
      const unsigned l=L_P0+w*16384;
      gv_issue(0,l); gv_issue(1,l+4096); VMC(4); gv_issue(2,l+8192); VMC(4); gv_issue(3,l+12288);
      gv_step(0,l); gv_step(1,l+4096); VMC(4); gv_step(2,l+8192); VMC(0); gv_step(3,l+12288);
      MFMA_DRAIN();
      #pragma unroll
      for(int j=0;j<4;j++){ PIN(ac[j]); if(lane<16) ldsw<float>(L_RED+w*256+j*64+lane*4,ac[j][0]); }
      LGKM(0);
      unsigned old=0; if(lane==0) old=__hip_atomic_fetch_add((LAS unsigned*)(size_t)L_CNT,1u,__ATOMIC_RELAXED,__HIP_MEMORY_SCOPE_WORKGROUP);
      if(__builtin_amdgcn_readfirstlane(old)==3){
        if(lane<16){ f32x4 s=ldsr<f32x4>(L_RED+lane*16)+ldsr<f32x4>(L_RED+256+lane*16)+ldsr<f32x4>(L_RED+512+lane*16)+ldsr<f32x4>(L_RED+768+lane*16);
          bs128<SC1>(__builtin_bit_cast(u32x4,s),mkrs(ws+OFF_MOD,49152),b*256+lane*16,0); }
        VMC(0);
        if(lane==0) sig(ws,FL_M+(b>>2)*128+(b&3)*4,tag); }
    }
    STAMPH(25);
    BAR_L();
    y_store();
    BAR_V(0);
    __builtin_amdgcn_s_sleep(1);
    poll4(ws,FL_M+min(lane,47)*128,tag);
    { rsrc_t rm=mkrs(ws+OFF_MOD,49152); u32x4 m0[5],m1[5]; const int t=tid-256;
      #pragma unroll
      for(int j=0;j<5;j++){ m0[j]=bl128<SC1>(rm,t*16,j*4096); m1[j]=bl128<SC1>(rm,t*16,24576+j*4096); }
      #pragma unroll
      for(int j=0;j<5;j++){ const f32x4 m=__builtin_bit_cast(f32x4,m0[j])+__builtin_bit_cast(f32x4,m1[j]); const unsigned p0=cvtpk(m[0],m[1]),p1=cvtpk(m[2],m[3]);
        f32x4 o={bflo(p0),bfhi(p0),bflo(p1),bfhi(p1)}; if(j==1||j==4) o+=1.f; ldsw<f32x4>(L_M+j*4096+t*16,o); } }
    STAMPH(24);
    BAR_L();
    if(wid==4){ poll4(ws,FL_1+xcd*8192+lane*128,tag); if(b==0 && lane==0) sig(ws,FL_E,tag); }
    BAR();
    if (wid<6){
      const int rot=(li+2*lj)&7, xrot=(((xcd&3)+2*(xcd>>2))&3)*8;
      rsrc_t ra=mkrs(ws+OFF_H2,S_*D_*2), rb=mkrs(w1,D_*F_*2);
      const unsigned va=pm*32*8192+w*4096+lane*16; unsigned vb[7];
      #pragma unroll
      for(int i=0;i<7;i++){ int e=(7*w+i)*64+lane; int row=e/28, c=e-row*28; c^=((row>>3)&1)<<1; vb[i]=row*(F_*2)+c*16+pj*448; }
      auto issA=[&](int t,int slot){ const int kt=((t&~7)+((t+rot)&7)+xrot)&31;
        #pragma unroll
        for(int i=0;i<4;i++) bl_lds<SC1>(ra,slot*G1_SLOT+w*4096+i*1024,va+i*1024,kt*8192); };
      auto issB=[&](int t,int slot){ const int kt=((t&~7)+((t+rot)&7)+xrot)&31;
        #pragma unroll
        for(int i=0;i<7;i++) bl_lds<0>(rb,slot*G1_SLOT+8192+(7*w+i)*1024,vb[i],kt*32*F_*2); };
      __builtin_amdgcn_s_sleep(2);
      #pragma unroll 1
      for(int t=0;t<3;t++) issB(t,t);
      BAR(); BAR(); BAR_V(0);
      #pragma unroll 1
      for(int t=0;t<3;t++) issA(t,t);
      issA(3,3); issB(3,3); BAR_V(19);
      issA(4,4); issB(4,4); issA(5,5); issB(5,5); BAR_V(37);
      issA(6,6); issB(6,6); BAR_V(44); issA(7,0); issB(7,0); BAR_V(44); issA(8,1); issB(8,1); BAR_V(44);
      int slot=2;
      #pragma unroll 1
      for(int s=4;s<G1_NT-5;s++){ issA(s+5,slot); issB(s+5,slot); slot=slot==G1_R-1?0:slot+1; BAR_V(44); }
      BAR_V(33); BAR_V(22); BAR_V(11); BAR_V(0); BAR();
    } else {
      #pragma unroll 1
      for(int s=1;s<G1_NT+5;s++){ BAR();
        if((TOUCH_W&2) && s==TOUCH_AT){ const unsigned o=(b*224+(w&1)*112+min(lane,55))*128; TOUCH(dummy,o,w2); const unsigned o2=o+56*128; TOUCH(dummy,o2,w2); } }
      asm volatile("s_waitcnt vmcnt(0)":"+v"(dummy)::"memory"); }
    { rsrc_t ra=mkrs(ws+OFF_HT,S_*F_*2), rb=mkrs(w2,D_*F_*2);
      const unsigned va=(pm*112+kh*56)*8192+w*2048+lane*16; unsigned vb[2];
      #pragma unroll
      for(int i=0;i<2;i++){ int e=(2*w+i)*64+lane; int row=e>>4,pos=e&15; int c=(((pos>>1)^(row&7))<<1)|(pos&1); vb[i]=(kh*1792+row)*2048+pn*256+c*16; }
      auto issA=[&](int kt,unsigned l){ bl_lds<SC1>(ra,l+w*2048,va,kt*8192); bl_lds<SC1>(ra,l+w*2048+1024,va+1024,kt*8192); };
      auto issB=[&](int kt,unsigned l){ bl_lds<0>(rb,l+8192+w*2048,vb[0],kt*65536); bl_lds<0>(rb,l+8192+w*2048+1024,vb[1],kt*65536); };
      #pragma unroll 1
      for(int u=0;u<7;u++) issB(pn*7+u,u*16384);
      BAR_V(0);
      BAR();
      const int rot=(li+2*lj)%7;
      int sa=pn+1, ja=rot, ca=0, sb=pn+1, jb=rot, cb2=0;
      auto nxt=[&](int& sl,int& j,int& c){ const int kt=(sl&7)*7+j; j= j==6? 0: j+1; if(++c==7){ c=0; sl++; } return kt; };
      #pragma unroll 1
      for(int u=0;u<8;u++) issB(nxt(sb,jb,cb2),u*16384);
      BAR();
      #pragma unroll 1
      for(int u=0;u<8;u++) issA(nxt(sa,ja,ca),u*16384);
      auto iss=[&](unsigned l){ const int kt=nxt(sa,ja,ca); issA(kt,l); issB(kt,l); };
      BAR_V(12); BAR_V(8);
      iss(0); BAR_V(8);
      iss(16384); iss(32768); BAR_V(12);
      #pragma unroll 1
      for(int s=2;s<21;s++){ iss(((2*s+7)&7)*16384); iss(((2*s+8)&7)*16384); BAR_V(12); }
      BAR_V(4); BAR_V(0);
      BAR(); BAR(); }
    return;
  }
  const int r8=lane&7, ub=w*8+(lane>>3), row=pm*128+pj*8+r8;
  TP_LOAD();
  unsigned ep, dv;
  { const unsigned z=0; asm volatile("global_load_dword %0, %1, %2 sc1":"=v"(ep):"v"(z),"s"(ws):"memory");
    const unsigned od=ch*2; ALOAD("global_load_ushort",dv,od,ssmD); }
  if(lane<32) ldsw<u32x4>(cb+CH_U+lane*16,(u32x4){0,0,0,0});
  if(tid==0) ldsw<unsigned>(L_CNT,0u);
  asm volatile("s_waitcnt vmcnt(0) lgkmcnt(0)\n\ts_barrier":"+v"(tp),"+v"(ep),"+v"(dv)::"memory");
  STAMP(1);
  const unsigned tag=__builtin_amdgcn_readfirstlane(ep)+1;
  p0_compute(tp);
  BAR_V(0);
  STAMP(2);
  auto stats_put=[&](const float* x,int slot){ float s=0,s2=0;
    #pragma unroll
    for(int i=0;i<32;i++){ s+=x[i]; s2+=x[i]*x[i]; }
    s=xsum8_16_32(s); s2=xsum8_16_32(s2); if(lane<8) ldsw<f32x2>(L_ST+slot*256+lane*32+w*8,(f32x2){s,s2}); };
  auto stats_get=[&](int slot,float& mu,float& rstd){ f32x4 a=ldsr<f32x4>(L_ST+slot*256+r8*32), c=ldsr<f32x4>(L_ST+slot*256+r8*32+16);
    mu=((a[0]+a[2])+(c[0]+c[2]))*(1.f/1024); const float var=fmaxf(((a[1]+a[3])+(c[1]+c[3]))*(1.f/1024)-mu*mu,0.f); rstd=__builtin_amdgcn_rsqf(var+1e-5f); };
  if(tid==0) sig(ws,FL_0+(xcd*32+idx)*128,tag);
  u32x4 qv[4];
  { const unsigned o=row*2048+ub*16;
    #pragma unroll
    for(int u=0;u<4;u++) ALOADO("global_load_dwordx4",qv[u],o,q,u*512); }
  asm volatile("s_waitcnt vmcnt(0)":"+v"(qv[0]),"+v"(qv[1]),"+v"(qv[2]),"+v"(qv[3])::"memory");
  { float x[32];
    #pragma unroll
    for(int u=0;u<4;u++) unpack8(qv[u],x+8*u);
    if(wid) stats_put(x,0);
    if(wid==0) { poll1(ws,FL_0+(xcd*32+(lane&31))*128,tag); }
  }
  BAR_L();
  STAMP(3);
  { rsrc_t ru=mkrs(ws+OFF_UT,S_*D_*2);
    #pragma unroll
    for(int i=0;i<4;i++) bl_lds<SC1>(ru,cb+CH_U+512+i*1024,ch*4096+i*1024+lane*16,0);
    const int r=lane&15,G=lane>>4;
    unsigned aB=cb+CH_U+32+32*r+16*G-128;
    const unsigned aA0=cb+((r&1)? CH_C0+2*(r+25-8*G) : CH_C1+2*(r+24-8*G));
    unsigned aA[4]={aA0,aA0+1024,aA0+2048,aA0+3072};
    f32x4 acc[8];
    #pragma unroll
    for(int n=0;n<8;n++){ acc[n]=(f32x4){0,0,0,0}; PIN(acc[n]); }
    u32x2 A[2][8][2]; u32x4 B[2][8];
    #define RDB(bf,pp,mm) RD128(B[bf][mm],aB,512*(mm)-64*(pp)+576)
    #pragma unroll
    for(int i=0;i<8;i++){ RD2(A[0][i][0],aA[i>>1],128*(i&1),128*(i&1)+1); RD2(A[0][i][1],aA[i>>1],128*(i&1)+2,128*(i&1)+3); }
    VMC(0);
    STAMP(4);
    #pragma unroll
    for(int i=0;i<8;i++) RDB(0,0,i);
    LGKM(0);
    #pragma unroll 1
    for(int it=0;it<4;it++){
      #pragma unroll
      for(int cu=0;cu<2;cu++){ const int nx=cu^1;
        int g=0;
        #pragma unroll
        for(int qq=0;qq<8;qq++){
          #pragma unroll
          for(int mm=0;mm+qq<8;mm++){
            MF16(acc[mm+qq],cat(A[cu][qq][0],A[cu][qq][1]),B[cu][mm]);
            if(g<24){ const int i=g/3, z=g%3;
              if(z==0) RD2(A[nx][i][0],aA[i>>1],16*(cu+1)+128*(i&1),16*(cu+1)+128*(i&1)+1);
              else if(z==1) RD2(A[nx][i][1],aA[i>>1],16*(cu+1)+128*(i&1)+2,16*(cu+1)+128*(i&1)+3);
              else RDB(nx,cu+1,i); }
            g++;
          } }
        LGKM(0); }
      aB-=128; aA[0]+=128; aA[1]+=128; aA[2]+=128; aA[3]+=128;
    }
    u32x2 uu[8];
    #pragma unroll
    for(int n=0;n<8;n++) uu[n]=ldsr<u32x2>(cb+CH_U+512+512*n+32*r+16*(G>>1)+8*(1-(G&1)));
    MFMA_DRAIN();
    const float Dv=bflo(dv);
    #pragma unroll
    for(int n=0;n<8;n++){ PIN(acc[n]); f32x4 y=acc[n];
      ldsw<u32x2>(cb+CH_C0+512*n+32*r+8*G,(u32x2){cvtpk(y[0]+bfhi(uu[n].y)*Dv,y[1]+bflo(uu[n].y)*Dv),cvtpk(y[2]+bfhi(uu[n].x)*Dv,y[3]+bflo(uu[n].x)*Dv)}); }
  }
  STAMP(5);
  BAR_L();
  y_store();
  if(wid==0){ float x[32];
    #pragma unroll
    for(int u=0;u<4;u++) unpack8(qv[u],x+8*u);
    stats_put(x,0); }
  BAR_VL(0);
  STAMP(6);
  if(wid==0 && lane<8) sig(ws,FL_1+lane*8192+(b>>2)*128+(b&3)*4,tag);
  u32x4 xo[4]; const unsigned tb=(pm*32*8+((row>>4)&7))*1024+(row&15)*16+(ub>>2)*8192+(ub&3)*256;
  { rsrc_t ry=mkrs(ws+OFF_YX,S_*D_*2);
    float mu,rstd; stats_get(0,mu,rstd);
    float x[32],gq[32];
    { const float nb=-mu*rstd;
      #pragma unroll
      for(int u=0;u<4;u++){ unpack8(qv[u],x+8*u);
        #pragma unroll
        for(int e=0;e<8;e++){ gq[8*u+e]=x[8*u+e]*rstd+nb; PIN(gq[8*u+e]); } } }
    BAR();
    #pragma unroll
    for(int u=0;u<8;u++){ const unsigned a=L_M+ub*32+(u>>1)*1024+(u&1)*16; const f32x4 sa=ldsr<f32x4>(a),ca=ldsr<f32x4>(a+4096),ga=ldsr<f32x4>(a+8192);
      #pragma unroll
      for(int e=0;e<4;e++){ gq[4*u+e]=ga[e]*(gq[4*u+e]*ca[e]+sa[e]); PIN(gq[4*u+e]); } }
    STAMP(22);
    BAR();
    STAMP(7);
    u32x2 yv[4][2];
    #pragma unroll
    for(int u=0;u<4;u++)
      #pragma unroll
      for(int z=0;z<2;z++) yv[u][z]=bl64<SC1>(ry,((row>>4)*256+2*ub)*128+(row&15)*8,(64*u+z)*128);
    asm volatile("":::"memory");
    f32x4 sb[8],cb_[8];
    #pragma unroll
    for(int u=0;u<8;u++){ const unsigned a=L_M+ub*32+(u>>1)*1024+(u&1)*16; sb[u]=ldsr<f32x4>(a+12288); cb_[u]=ldsr<f32x4>(a+16384); PIN(sb[u]); PIN(cb_[u]); }
    rsrc_t rh=mkrs(ws+OFF_H2,S_*D_*2);
    #pragma unroll
    for(int u=0;u<4;u++){ float y[8]; unpack8(cat(yv[u][0],yv[u][1]),y);
      #pragma unroll
      for(int e=0;e<8;e++) x[8*u+e]+=gq[8*u+e]*y[e];
      xo[u]=pack8(x+8*u); unpack8(xo[u],x+8*u); }
    stats_put(x,1);
    BAR_L();
    stats_get(1,mu,rstd); const float nb=-mu*rstd;
    #pragma unroll
    for(int u=0;u<4;u++){ float h[8];
      #pragma unroll
      for(int e=0;e<8;e++) h[e]=(x[8*u+e]*rstd+nb)*cb_[2*u+(e>>2)][e&3]+sb[2*u+(e>>2)][e&3];
      bs128<SC1>(pack8(h),rh,tb,u*65536); }
  }
  STAMP(8);
  BAR_V(0);
  STAMP(9);
  if(tid==0) sig(ws,FL_2+(pm*16+pj)*128,tag);
  if(wid) __builtin_amdgcn_s_sleep(1);
  { rsrc_t rx=mkrs(ws+OFF_X1,S_*D_*2);
    #pragma unroll
    for(int u=0;u<4;u++) bs128<SC1>(xo[u],rx,tb,u*65536); }
  if(wid==0) poll1(ws,FL_2+(pm*16+(lane&15))*128,tag);
  BAR();
  STAMP(10);
  const int wm=wid>>1, wn=wid&1, G=lane>>4, q4=(lane>>2)&3, p4=lane&3;
  { const int sw=(G&1)*32; const unsigned kp=8192+(8*G+q4)*448+8*p4;
    const unsigned aA=wm*4096+lane*16, aE=kp+256*wn+sw, aO=kp+256*wn-sw, aS=kp+(wn? 224-sw : 192+sw);
    f32x4 acc[7][4];
    #pragma unroll
    for(int j=0;j<7;j++)
      #pragma unroll
      for(int i=0;i<4;i++){ acc[j][i]=(f32x4){0,0,0,0}; PIN(acc[j][i]); }
    u32x4 X[2][4]; u32x2 W[2][7][2];
    auto rd=[&](int bf,int rr,unsigned a,unsigned e,unsigned o,unsigned sgl){
      const int z= rr==0?0 : rr<3? 3+rr : rr<6? rr-2 : rr;
      if(z<4) RD128(X[bf][z],a,z*1024);
      else { const int j=(z-4)>>1,t=(z-4)&1; if(j<6) RDTR(W[bf][j][t],(j&1)?o:e,32*j+1792*t); else RDTR(W[bf][6][t],sgl,1792*t); } };
    auto step=[&](int cu,bool more,unsigned so){
      const unsigned a=aA+so,e=aE+so,o=aO+so,sgl=aS+so;
      #pragma unroll
      for(int g=0;g<28;g++){ const int j=g>>2,i=g&3;
        MF16(acc[j][i],cat(W[cu][j][0],W[cu][j][1]),X[cu][i]);
        if(more && g<18) rd(cu^1,g,a,e,o,sgl); }
      LGKM(0); };
    BAR();
    STAMP(11);
    #pragma unroll
    for(int i=0;i<18;i++) rd(0,i,aA,aE,aO,aS);
    LGKM(0);
    unsigned so=G1_SLOT;
    #pragma unroll 1
    for(int s=0;s<G1_NT-2;s+=2){
      BAR(); step(0,true,so); so+=G1_SLOT; if(so==G1_R*G1_SLOT) so=0;
      BAR(); step(1,true,so); so+=G1_SLOT; if(so==G1_R*G1_SLOT) so=0; }
    BAR(); step(0,true,so); BAR(); step(1,false,0);
    MFMA_DRAIN();
    STAMP(12);
    rsrc_t rs=mkrs(ws+OFF_HT,S_*F_*2);
    const unsigned hb=((pm*112+pj*7)*8+wm*4)*1024+lane*16, lh=wm*4096+lane*16;
    auto act=[&](int j,int i){ PIN(acc[j][i]); const f32x4 v_=acc[j][i]; return (u32x2){cvtpk(silu_f(v_[0]),silu_f(v_[1])),cvtpk(silu_f(v_[2]),silu_f(v_[3]))}; };
    #pragma unroll
    for(int u=0;u<3;u++)
      #pragma unroll
      for(int i=0;i<4;i++){ const u32x4 o=cat(act(2*u,i),act(2*u+1,i)); bs128<SC1>(o,rs,hb+wn*32768+i*1024,u*8192); ldsw<u32x4>(lh+wn*65536+u*16384+i*1024,o); }
    #pragma unroll
    for(int i=0;i<4;i++){ const u32x2 o=act(6,i); bs64<SC1>(o,rs,hb+wn*8+i*1024,3*8192); ldsw<u32x2>(lh+wn*8+3*16384+i*1024,o); }
  }
  STAMP(13);
  { const int kk=4*G+q4; const unsigned aA=wm*4096+lane*16; unsigned aW[4];
    #pragma unroll
    for(int j=0;j<4;j++){ const int jt=(((j>>1)^kh)*4+2*wn+(j&1)); aW[j]=8192+kk*256+((jt^(kk&7))*32)+8*p4; }
    f32x4 acc[4][4];
    #pragma unroll
    for(int j=0;j<4;j++)
      #pragma unroll
      for(int i=0;i<4;i++){ acc[j][i]=(f32x4){0,0,0,0}; PIN(acc[j][i]); }
    u32x4 X[2][4]; u32x2 W[2][4][2];
    auto rd=[&](int bf,int rr,unsigned a,const unsigned* e,int off){
      const int z= rr==0?0 : rr<3? 3+rr : rr<6? rr-2 : rr;
      if(z<4) RD128(X[bf][z],a,off+z*1024); else { const int j=(z-4)>>1,t=(z-4)&1; RDTR(W[bf][j][t],e[j],off+4096*t); } };
    auto unit=[&](int cu,bool more,unsigned so,int off){
      const unsigned a=aA+so, e[4]={aW[0]+so,aW[1]+so,aW[2]+so,aW[3]+so};
      #pragma unroll
      for(int g=0;g<16;g++){ const int j=g>>2,i=g&3;
        if(i==0){ if(more) LGKM(6+2*j); else LGKM(6-2*j); }
        MF16(acc[j][i],cat(W[cu][j][0],W[cu][j][1]),X[cu][i]);
        if(more && g<12) rd(cu^1,g,a,e,off); } };
    #pragma unroll 1
    for(int ph=0;ph<2;ph++){
      BAR_L();
      STAMP(14+3*ph);
      #pragma unroll
      for(int i=0;i<12;i++) rd(0,i,aA,aW,0);
      const int n=ph? 24:3;
      #pragma unroll 1
      for(int s=0;s<n;s++){ unit(0,true,(s&3)*32768,16384); if(ph) BAR(); unit(1,true,((s+1)&3)*32768,0); }
      unit(0,false,0,0);
      if(ph==0){
        BAR_V(0);
        STAMP(15);
        if(wid==0){ if(lane==0) sig(ws,FL_3+(pm*16+pj)*128,tag); poll1(ws,FL_3+(pm*16+kh*8+(lane&7))*128,tag); }
        BAR();
        STAMP(16);
      } }
    MFMA_DRAIN();
    STAMP(18);
    rsrc_t rx=mkrs(ws+OFF_XCH,8u<<20);
    const unsigned me=(pm*8+pn)*2+kh;
    #pragma unroll
    for(int j=2;j<4;j++)
      #pragma unroll
      for(int i=0;i<4;i++){ PIN(acc[j][i]); bs128<SC1>(__builtin_bit_cast(u32x4,acc[j][i]),rx,me*32768+wid*8192+lane*16,((j-2)*4+i)*1024); }
    rsrc_t r1=mkrs(ws+OFF_X1,S_*D_*2), rm=mkrs(ws+OFF_MOD,49152);
    const int c0=pn*128+kh*64+wn*32; u32x2 xv[2][4],gbv[2];
    #pragma unroll
    for(int j=0;j<2;j++){ { const f32x4 m=__builtin_bit_cast(f32x4,bl128<SC1>(rm,(5*1024+c0+4*G)*4,64*j))+__builtin_bit_cast(f32x4,bl128<SC1>(rm,(5*1024+c0+4*G)*4,24576+64*j)); gbv[j]=(u32x2){cvtpk(m[0],m[1]),cvtpk(m[2],m[3])}; }
      #pragma unroll
      for(int i=0;i<4;i++) xv[j][i]=bl64<SC1>(r1,((pm*32+(c0>>5))*8+wm*4)*1024+(G>>1)*256+(lane&15)*16+(G&1)*8,i*1024+j*512); }
    BAR_V(0);
    STAMP(19);
    if(wid==0){ if(lane==0) sig(ws,FL_4+me*128,tag); poll1(ws,FL_4+(me^1)*128,tag); }
    BAR();
    STAMP(20);
    u32x4 pv[2][4];
    #pragma unroll
    for(int j=0;j<2;j++)
      #pragma unroll
      for(int i=0;i<4;i++) pv[j][i]=bl128<SC1>(rx,(me^1)*32768+wid*8192+lane*16,(j*4+i)*1024);
    rsrc_t ro=mkrs(out,S_*D_*2);
    #pragma unroll
    for(int i=0;i<4;i++){ u32x2 o[2];
      #pragma unroll
      for(int j=0;j<2;j++){ PIN(acc[j][i]); f32x4 v_=acc[j][i]+__builtin_bit_cast(f32x4,pv[j][i]);
        o[j].x=cvtpk(bflo(xv[j][i].x)+bflo(gbv[j].x)*v_[0],bfhi(xv[j][i].x)+bfhi(gbv[j].x)*v_[1]);
        o[j].y=cvtpk(bflo(xv[j][i].y)+bflo(gbv[j].y)*v_[2],bfhi(xv[j][i].y)+bfhi(gbv[j].y)*v_[3]); }
      auto sx=__builtin_amdgcn_permlane16_swap(o[0].x,o[1].x,false,false); auto sy=__builtin_amdgcn_permlane16_swap(o[0].y,o[1].y,false,false);
      bs128<SC1>((u32x4){sx[0],sy[0],sx[1],sy[1]},ro,(pm*128+wm*64+(lane&15))*2048+(c0+16*(G&1)+8*(G>>1))*2,i*32768); }
    STAMP(21);
  }
}

extern "C" void kernel_launch(void* const* d_in, const int* in_sizes, int n_in, void* d_out, int out_size, void* d_ws, size_t ws_size, hipStream_t stream){
  (void)hipFuncSetAttribute((const void*)k_all,hipFuncAttributeMaxDynamicSharedMemorySize,LDS_BYTES);
  k_all<<<256,512,LDS_BYTES,stream>>>((const u16*)d_in[0],(const u16*)d_in[1],(const u16*)d_in[2],(const u16*)d_in[3],(const u16*)d_in[4],(const u16*)d_in[5],
                                      (const u16*)d_in[7],(const u16*)d_in[8],(const u16*)d_in[9],(const u16*)d_in[10],(char*)d_ws,(u16*)d_out);
}
```

```cpp
#include <hip/hip_runtime.h>
#include <hip/hip_bf16.h>
typedef unsigned short u16;
typedef unsigned long long u64;
typedef __bf16 bf16x2 __attribute__((ext_vector_type(2)));
typedef float f32x2 __attribute__((ext_vector_type(2)));
typedef float f32x4 __attribute__((ext_vector_type(4)));
typedef float f32x16 __attribute__((ext_vector_type(16)));
typedef float f32x32 __attribute__((ext_vector_type(32)));
typedef unsigned u32x2 __attribute__((ext_vector_type(2)));
typedef unsigned u32x4 __attribute__((ext_vector_type(4)));
typedef unsigned u32x16 __attribute__((ext_vector_type(16)));
#define LAS __attribute__((address_space(3)))
#define GAS __attribute__((address_space(1)))
#define DI __device__ __forceinline__
#define HPAD 0
constexpr int S_=2048, D_=1024, F_=3584;
constexpr unsigned OFF_FLAG=0, OFF_MOD=589824, OFF_STAMP=655360, OFF_UT=1u<<20, OFF_YX=OFF_UT+(4u<<20), OFF_H2=OFF_YX+(4u<<20), OFF_X1=OFF_H2+(4u<<20),
                   OFF_XCH=OFF_X1+(4u<<20), OFF_HT=OFF_XCH+(8u<<20), HT_PM=112*8192+HPAD, WS_END=OFF_HT+16*HT_PM;
constexpr unsigned FL_E=0, FL_M=4096, FL_0=32768, FL_1=65536, FL_2=131072, FL_3=163840, FL_4=FL_3+262144;
constexpr int SC1=16;
typedef __amdgpu_buffer_rsrc_t rsrc_t;
DI rsrc_t mkrs(const void* p, unsigned n){ return __builtin_amdgcn_make_buffer_rsrc((void*)p,0,n,0x00020000); }
DI unsigned cvtpk(float lo, float hi){ f32x2 v={lo,hi}; bf16x2 b=__builtin_convertvector(v,bf16x2); return __builtin_bit_cast(unsigned,b); }
DI float bflo(unsigned u){ return __uint_as_float(u<<16); }
DI float bfhi(unsigned u){ return __uint_as_float(u&0xffff0000u); }
DI float rbf(float x){ return bflo(cvtpk(x,x)); }
DI float silu_f(float x){ return x*__builtin_amdgcn_rcpf(1.f+__builtin_amdgcn_exp2f(x*-1.44269504089f)); }
template<int AUX> DI void bl_lds(rsrc_t rs, unsigned ldsoff, unsigned voff, unsigned soff){
  __builtin_amdgcn_raw_ptr_buffer_load_lds(rs,(LAS void*)(size_t)ldsoff,16,voff,soff,0,AUX); }
template<int AUX> DI u32x4 bl128(rsrc_t rs, unsigned voff, unsigned soff){ return __builtin_amdgcn_raw_buffer_load_b128(rs,voff,soff,AUX); }
template<int AUX> DI u32x2 bl64(rsrc_t rs, unsigned voff, unsigned soff){ return __builtin_amdgcn_raw_buffer_load_b64(rs,voff,soff,AUX); }
template<int AUX> DI void bs128(u32x4 v, rsrc_t rs, unsigned voff, unsigned off){ __builtin_amdgcn_raw_buffer_store_b128(v,rs,voff+off,0,AUX); }
template<int AUX> DI void bs64(u32x2 v, rsrc_t rs, unsigned voff, unsigned off){ __builtin_amdgcn_raw_buffer_store_b64(v,rs,voff+off,0,AUX); }
template<int AUX> DI void bs32(unsigned v, rsrc_t rs, unsigned voff, unsigned soff){ __builtin_amdgcn_raw_buffer_store_b32(v,rs,voff,soff,AUX); }
#define MF16(acc,a,b) asm volatile("v_mfma_f32_16x16x32_bf16 %0, %1, %2, %0" : "+v"(acc) : "v"(a), "v"(b))
#define RD128(d,a,o) asm volatile("ds_read_b128 %0, %1 offset:%2" : "=v"(d) : "v"(a), "i"(o))
#define RDTR(d,a,o)  asm volatile("ds_read_b64_tr_b16 %0, %1 offset:%2" : "=v"(d) : "v"(a), "i"(o))
#define RD2(d,a,o0,o1) asm volatile("ds_read2_b32 %0, %1 offset0:%2 offset1:%3" : "=v"(d) : "v"(a), "i"(o0), "i"(o1))
#define LGKM(n) asm volatile("s_waitcnt lgkmcnt(%0)"::"i"(n):"memory")
#define VMC(n) asm volatile("s_waitcnt vmcnt(%0)"::"i"(n):"memory")
#define BAR() asm volatile("s_barrier":::"memory")
#define BAR_L() asm volatile("s_waitcnt lgkmcnt(0)\n\ts_barrier":::"memory")
#define BAR_V(n) asm volatile("s_waitcnt vmcnt(%0)\n\ts_barrier"::"i"(n):"memory")
#define BAR_VL(n) asm volatile("s_waitcnt vmcnt(%0) lgkmcnt(0)\n\ts_barrier"::"i"(n):"memory")
#define MFMA_DRAIN() asm volatile("s_nop 7\n\ts_nop 7":::"memory")
#define PIN(x) asm volatile("":"+v"(x))
template<class V> DI f32x4 q4of(const V& v,int i){ return (f32x4){v[4*i],v[4*i+1],v[4*i+2],v[4*i+3]}; }
DI int lane_id(){ int l; asm volatile("v_mbcnt_lo_u32_b32 %0, -1, 0\n\tv_mbcnt_hi_u32_b32 %0, -1, %0":"=v"(l)); return l; }
DI float addf(float a,float b){ float r; asm("v_add_f32 %0, %1, %2":"=v"(r):"v"(a),"v"(b)); return r; }
DI f32x4 add4(f32x4 a,f32x4 b){ return (f32x4){addf(a[0],b[0]),addf(a[1],b[1]),addf(a[2],b[2]),addf(a[3],b[3])}; }
DI u32x4 cat(u32x2 a,u32x2 b){ return (u32x4){a.x,a.y,b.x,b.y}; }
template<class T> DI T ldsr(unsigned a){ return *(LAS T*)(size_t)a; }
template<class T> DI void ldsw(unsigned a, T v){ *(LAS T*)(size_t)a=v; }
DI float xsum8_16_32(float x){
  x+=__uint_as_float(__builtin_amdgcn_update_dpp(0u,__float_as_uint(x),0x128,0xf,0xf,false));
  { unsigned u=__float_as_uint(x); auto r=__builtin_amdgcn_permlane16_swap(u,u,false,false); const unsigned a=r[0],c=r[1]; x=__uint_as_float(a)+__uint_as_float(c); }
  { unsigned u=__float_as_uint(x); auto r=__builtin_amdgcn_permlane32_swap(u,u,false,false); const unsigned a=r[0],c=r[1]; x=__uint_as_float(a)+__uint_as_float(c); }
  return x; }
DI void unpack8(u32x4 v,float* f){ f[0]=bflo(v.x); f[1]=bfhi(v.x); f[2]=bflo(v.y); f[3]=bfhi(v.y); f[4]=bflo(v.z); f[5]=bfhi(v.z); f[6]=bflo(v.w); f[7]=bfhi(v.w); }
DI u32x4 pack8(const float* f){ return (u32x4){cvtpk(f[0],f[1]),cvtpk(f[2],f[3]),cvtpk(f[4],f[5]),cvtpk(f[6],f[7])}; }
#define POLL_SLEEP 1
DI void sig(rsrc_t rf, unsigned off, unsigned val){ bs32<SC1>(val,rf,off,0); }
DI bool late(u64 t0){ return __builtin_amdgcn_s_memrealtime()-t0>2000000ull; }
template<unsigned MASK> DI void poll1(rsrc_t rf, unsigned voff, unsigned val){
  const u64 t0=__builtin_amdgcn_s_memrealtime();
  for(;;){ unsigned v; asm volatile("buffer_load_dword %0, %1, %2, 0 offen sc1\n\ts_waitcnt vmcnt(0)":"=&v"(v):"v"(voff),"s"(rf):"memory");
    if(((v^val)&MASK)==0 || late(t0)) break; __builtin_amdgcn_s_sleep(POLL_SLEEP); } }
DI void poll4(rsrc_t rf, unsigned voff, unsigned val){
  const u64 t0=__builtin_amdgcn_s_memrealtime();
  for(;;){ u32x4 v; asm volatile("buffer_load_dwordx4 %0, %1, %2, 0 offen sc1\n\ts_waitcnt vmcnt(0)":"=&v"(v):"v"(voff),"s"(rf):"memory");
    if((v.x==val && v.y==val && v.z==val && v.w==val) || late(t0)) break; __builtin_amdgcn_s_sleep(POLL_SLEEP); } }
#define SA(i)
#define SA_OPS
#define SA_CLOB
#define STAMP_DUMP(lo,hi)
#define STAMP(i)
#define STAMPH(i)

#define MOD_F32
#define TLBX 0
#define TLBW 1
#define TLBS 0
#define XE 0
#define BAUX 0
#define PF1 3
#define EP 1
#define T0 4
#define T1 8
#define T2 28
#define T3 28
#define OUTAUX SC1
#define F0_SLEEP 8
#define F2_SLEEP 12
#ifndef M_SLEEP
#define M_SLEEP 2
#endif
#define F1_SLEEP 32
#define H_SLEEP 30
#define NTK 0
#define NTS ""
#define ORD MAP
#define SKU 37
#define SKY 37
#define MAP 0
#define SIG2 2
#define ABL 0
#define SIGP 7
#define PF_SLEEP 10
#define GVMODE 2
#define NTA 2
#define TOUCH_SLEEP 6
DI u32x2 silu4(f32x2 a,f32x2 b){
  f32x2 t,u; const f32x2 c={-1.44269504089f,-1.44269504089f}, one={1.f,1.f};
  asm("v_pk_mul_f32 %0, %1, %2":"=v"(t):"v"(a),"s"(c)); asm("v_pk_mul_f32 %0, %1, %2":"=v"(u):"v"(b),"s"(c));
  t=(f32x2){__builtin_amdgcn_exp2f(t.x),__builtin_amdgcn_exp2f(t.y)}; u=(f32x2){__builtin_amdgcn_exp2f(u.x),__builtin_amdgcn_exp2f(u.y)};
  t+=one; u+=one;
  t=(f32x2){__builtin_amdgcn_rcpf(t.x),__builtin_amdgcn_rcpf(t.y)}; u=(f32x2){__builtin_amdgcn_rcpf(u.x),__builtin_amdgcn_rcpf(u.y)};
  t*=a; u*=b; return (u32x2){cvtpk(t.x,t.y),cvtpk(u.x,u.y)}; }
DI u32x4 tagged(f32x4 v,unsigned tag){ const u32x4 u=__builtin_bit_cast(u32x4,v); return (u32x4){__builtin_amdgcn_perm(u.x,tag,0x07060500),__builtin_amdgcn_perm(u.y,tag,0x07060500),__builtin_amdgcn_perm(u.z,tag,0x07060500),__builtin_amdgcn_perm(u.w,tag,0x07060500)}; }
DI unsigned untag(unsigned m,u32x4 u,unsigned tag){ return m|(u.x^tag)|(u.y^tag)|(u.z^tag)|(u.w^tag); }
constexpr int CODE_LINES=7, CODE_BYTES=27648;

constexpr int CH_B=128, CH_U=0, CH_C0=4608, CH_C1=CH_C0+4160, CH_STR=CH_C1+4160;
constexpr int L_SC=CH_B+4*CH_STR, L_RED=L_SC+1024, L_P0=57344, L_Q=L_P0+49152, L_M=137216, L_ST=157696, L_CNT=L_ST+512, L_SCR=L_ST+1024;
constexpr int G1_SLOT=22528, G1_R=7, G1_NT=32, LDS_BYTES=163840;
constexpr int G2_RING=9*16384;
#define ALOAD(ins,d,o,p) asm volatile(ins " %0, %1, %2":"=v"(d):"v"(o),"s"(p):"memory")
#define BLOAD(ins,d,o,i) asm volatile(ins " %0, %1, %2, 0 offen offset:%3 sc1":"=v"(d):"v"(o),"s"(rf),"i"(i):"memory")
DI void touch_lines(rsrc_t rs,unsigned voff){ __builtin_amdgcn_raw_ptr_buffer_load_lds(rs,(LAS void*)(size_t)L_SCR,4,voff,0,0,0); }
#define ALOADO(ins,d,o,p,i) asm volatile(ins " %0, %1, %2 offset:%3":"=v"(d):"v"(o),"s"(p),"i"(i):"memory")

__global__ __launch_bounds__(512,2) void k_all(const u16* k,const u16* v,const u16* shk,const u16* ssmD,const u16* cvec,char* ws,const u16* ssmk,const u16* q,
                                               const u16* w1,const u16* w2,const u16* wmod,u16* out){
  u64 pc0; asm volatile("s_getpc_b64 %0":"=s"(pc0));
  const int wid=__builtin_amdgcn_readfirstlane(threadIdx.x>>6), b=blockIdx.x; int lane=threadIdx.x&63;
  const int xcd=b&7, idx=b>>3;
  const int li=idx&7, lj=idx>>3, pm=(xcd>>2)*8+li, pj=(xcd&3)*4+lj, rot=(li+lj)&3, xrot=(((xcd&3)+2*(xcd>>2))&3)*8;
  const int pn=pj&7, kh=pj>>3;
  const rsrc_t rf=mkrs(ws,WS_END);
  const int cg=xcd*32+idx;
  const int w=wid&3, ch=cg*4+w; const unsigned cb=CH_B+w*CH_STR;
  STAMP(0);
  const bool gv=b<192; const int gc= b<96? b : b-96, gr=(b<96? 0:512)+w*128;
  u32x2 tp; unsigned ep, dv, cv;
  { const unsigned o=(xcd*128+wid*16+(lane&15))*8; ALOAD("global_load_dwordx2",tp,o,shk);
    if(wid==7) touch_lines(mkrs((const void*)(pc0&~255ull),CODE_BYTES),(idx*CODE_LINES+min(lane,CODE_LINES-1))*128);
    const unsigned z=0; asm volatile("global_load_dword %0, %1, %2 sc1":"=v"(ep):"v"(z),"s"(ws):"memory");
    const unsigned od=ch*2; ALOAD("global_load_ushort",dv,od,ssmD); }
  { const unsigned oc=(gr+lane*2)*2; ALOAD("global_load_dword",cv,oc,cvec); }
  { const int s0=idx*64, pos=lane&15, lr=lane>>4;
    rsrc_t rk=mkrs(k,S_*D_*2), rv=mkrs(v,S_*D_*2);
    const int f=lr|((wid>>1&1)<<2), c=(((pos>>1)^f)<<1)|(pos&1);
    const unsigned vo=(s0+4*wid+lr)*2048+xcd*256+c*16;
    bl_lds<NTK>(rk,L_P0+wid*1024,vo-8192,0); bl_lds<NTK>(rv,L_P0+17408+wid*1024,vo,0);
    bl_lds<NTK>(rk,L_P0+wid*1024+8192,vo+57344,0); bl_lds<NTK>(rv,L_P0+17408+wid*1024+8192,vo,65536);
    if(wid==0) bl_lds<NTK>(rk,L_P0+16384,vo+57344,65536);
  }
  if(lane<32) ldsw<u32x4>(cb+CH_U+lane*16,(u32x4){0,0,0,0});
  if((wid==0 && lane==0)) ldsw<u32x2>(L_CNT,(u32x2){0u,0u});
  u32x4 hv[4];
  __builtin_amdgcn_s_sleep(H_SLEEP);
  { rsrc_t rh=mkrs(ssmk,wid>=4? S_*D_*2:0); const unsigned o=ch*4096+lane*16;
    #pragma unroll
    for(int i=0;i<4;i++) asm volatile("buffer_load_dwordx4 %0, %1, %2, 0 offen offset:%3":"=v"(hv[i]):"v"(o),"s"(rh),"i"(i*1024):"memory"); }
  unsigned a0,a1,so0;
  { const int pos=lane&15, lr=lane>>4, qq=pos>>2, p=pos&3, par=(lr&1)<<2;
    a0=L_P0+(8*lr+qq)*256+((wid^(qq|par))*32)+p*8; a1=L_P0+(8*lr+qq)*256+((wid^(qq|(par^4)))*32)+p*8;
    so0=OFF_UT+(idx*1024+((xcd*128+wid*16+pos+idx*SKU)&1023))*128+lr*16; }
  asm volatile("s_waitcnt vmcnt(4) lgkmcnt(0)\n\ts_barrier":"+v"(tp),"+v"(ep),"+v"(dv),"+v"(cv),"+v"(a0),"+v"(a1),"+v"(so0)::"memory");
  STAMP(1);
  const unsigned tag=__builtin_amdgcn_readfirstlane(ep)+1;
  {
    u32x2 kk[2][3],vv[2][2];
    #pragma unroll
    for(int z=0;z<2;z++){ RDTR(kk[z][0],a0,z*8192); RDTR(kk[z][1],a0,z*8192+1024); RDTR(kk[z][2],a1,z*8192+2048); RDTR(vv[z][0],a0,17408+z*8192); RDTR(vv[z][1],a0,17408+z*8192+1024); }
    asm volatile("s_waitcnt lgkmcnt(0)":"+v"(kk[0][0]),"+v"(kk[0][1]),"+v"(kk[0][2]),"+v"(kk[1][0]),"+v"(kk[1][1]),"+v"(kk[1][2]),"+v"(vv[0][0]),"+v"(vv[0][1]),"+v"(vv[1][0]),"+v"(vv[1][1])::"memory");
    const float w0=bflo(tp.x),w1_=bfhi(tp.x),w2_=bflo(tp.y),w3=bfhi(tp.y);
    f32x2 kf[12],u[8];
    #pragma unroll
    for(int i=1;i<12;i++) kf[i]= (i&1)? (f32x2){bfhi(kk[0][i>>2][(i>>1)&1]),bfhi(kk[1][i>>2][(i>>1)&1])} : (f32x2){bflo(kk[0][i>>2][(i>>1)&1]),bflo(kk[1][i>>2][(i>>1)&1])};
    #pragma unroll
    for(int i=0;i<8;i++){ const f32x2 vf= (i&1)? (f32x2){bfhi(vv[0][i>>2][(i>>1)&1]),bfhi(vv[1][i>>2][(i>>1)&1])} : (f32x2){bflo(vv[0][i>>2][(i>>1)&1]),bflo(vv[1][i>>2][(i>>1)&1])};
      u[i]=(((kf[i+1]*w3+kf[i+2]*w2_)+kf[i+3]*w1_)+kf[i+4]*w0)*vf; }
    #pragma unroll
    for(int z=0;z<2;z++) bs128<SC1>((u32x4){cvtpk(u[7][z],u[6][z]),cvtpk(u[5][z],u[4][z]),cvtpk(u[3][z],u[2][z]),cvtpk(u[1][z],u[0][z])},rf,so0,z*64); }
  asm volatile("s_waitcnt vmcnt(0)\n\ts_barrier":"+v"(hv[0]),"+v"(hv[1]),"+v"(hv[2]),"+v"(hv[3])::"memory");
  STAMP(2);
  auto y_store=[&](){
    const int t=wid*64+lane; unsigned a=CH_B+CH_C0+t*4, o0=OFF_YX+((t>>3)*256+((cg+(t>>3)*SKY)&255))*128+(t&7)*16, o1=OFF_YX+(((t>>3)+64)*256+((cg+((t>>3)+64)*SKY)&255))*128+(t&7)*16, y[8];
    asm volatile("s_waitcnt vmcnt(0) lgkmcnt(0)\n\ts_barrier\n\t"
      "ds_read_b32 %0, %8\n\tds_read_b32 %1, %8 offset:%11\n\tds_read_b32 %2, %8 offset:%12\n\tds_read_b32 %3, %8 offset:%13\n\t"
      "ds_read_b32 %4, %8 offset:2048\n\tds_read_b32 %5, %8 offset:%11+2048\n\tds_read_b32 %6, %8 offset:%12+2048\n\tds_read_b32 %7, %8 offset:%13+2048\n\ts_waitcnt lgkmcnt(0)"
      :"=&v"(y[0]),"=&v"(y[1]),"=&v"(y[2]),"=&v"(y[3]),"=&v"(y[4]),"=&v"(y[5]),"=&v"(y[6]),"=&v"(y[7]),"+v"(a),"+v"(o0),"+v"(o1):"i"(CH_STR),"i"(2*CH_STR),"i"(3*CH_STR):"memory");
    #pragma unroll
    for(int z=0;z<2;z++) bs128<SC1>((u32x4){__builtin_amdgcn_perm(y[4*z+1],y[4*z],0x05040100),__builtin_amdgcn_perm(y[4*z+3],y[4*z+2],0x05040100),__builtin_amdgcn_perm(y[4*z+1],y[4*z],0x07060302),__builtin_amdgcn_perm(y[4*z+3],y[4*z+2],0x07060302)},rf,z? o1:o0,0); };

  if (wid>=4){
    __builtin_amdgcn_s_setprio(3);
    rsrc_t rw=mkrs(wmod,1024*6144*2);
    const unsigned vo0=(gr+(lane>>3))*12288+gc*128+((((lane>>1)&3)^((lane>>4)&1))*2+(lane&1))*16, vo1=vo0^64;
    auto gv_issue=[&](int st,unsigned l){
      #pragma unroll
      for(int i=0;i<4;i++) bl_lds<NTA>(rw,l+i*1024,(i&1)? vo1:vo0,(st*32+i*8)*12288); };
    f32x4 ac[4];
    auto gv_step=[&](int st,unsigned l){
      const int G=lane>>4,q4=(lane>>2)&3,gg=((q4>>1)&1)|((G&1)<<1);
      const unsigned aw0=l+(8*G+q4)*128+(lane&3)*8, as=L_SC+w*256+G*16+st*64;
      u32x4 sv; u32x2 t[4][2];
      RD128(sv,as,0);
      #pragma unroll
      for(int j=0;j<4;j++){ const unsigned a=aw0+((j^gg)*32); RDTR(t[j][0],a,0); RDTR(t[j][1],a,512); }
      LGKM(0);
      #pragma unroll
      for(int j=0;j<4;j++) MF16(ac[j],sv,cat(t[j][0],t[j][1])); };
    const unsigned l=L_P0+w*12288;
    if(TLBX && wid==5){ __builtin_amdgcn_s_sleep(1); touch_lines(rf,lane*(1u<<20)+(512u<<10)); }
    if(TLBW && wid==6){
      const int n=lane; const char* p= n<4? (const char*)out : n<11? (const char*)w1 : n<18? (const char*)w2 : n<30? (const char*)wmod : (const char*)q; const int o= n<4? n : n<11? n-4 : n<18? n-11 : n<30? n-18 : min(n-30,3);
      __builtin_amdgcn_s_sleep(1);
      if(TLBW==1 || !(o&1)) __builtin_amdgcn_global_load_lds((const GAS unsigned*)(p+o*(1u<<20)+(TLBS? idx*128:0)),(LAS unsigned*)(size_t)L_SCR,4,0,0); }
    ldsw<unsigned>(L_SC+w*256+lane*4,cvtpk(silu_f(bflo(cv)),silu_f(bfhi(cv))));
    #pragma unroll
    for(int i=0;i<4;i++){ const unsigned a=cb+32+i*1024+lane*16;
      unsigned nx=__builtin_amdgcn_update_dpp(0u,hv[i].x,0x130,0xf,0xf,true);
      if(i<3){ const unsigned f=__builtin_amdgcn_readfirstlane(hv[i<3?i+1:i].x); nx= lane==63? f: nx; }
      ldsw<u32x4>(a+CH_C0,hv[i]);
      ldsw<u32x4>(a+CH_C1,(u32x4){__builtin_amdgcn_alignbit(hv[i].y,hv[i].x,16),__builtin_amdgcn_alignbit(hv[i].z,hv[i].y,16),__builtin_amdgcn_alignbit(hv[i].w,hv[i].z,16),__builtin_amdgcn_alignbit(nx,hv[i].w,16)}); }
    { unsigned h0=__builtin_amdgcn_readfirstlane(hv[0].x); if(lane<8){ ldsw<unsigned>(cb+CH_C0+lane*4,0u); ldsw<unsigned>(cb+CH_C1+lane*4, lane==7? h0<<16 : 0u); } }
    #pragma unroll
    for(int j=0;j<4;j++){ ac[j]=(f32x4){0,0,0,0}; PIN(ac[j]); }
    BAR_L();
    auto q_issue=[&](){ rsrc_t rq=mkrs(q,S_*D_*2); const unsigned o=(pm*128+pj*8+(lane&7))*2048+(w*8+(lane>>3))*16;
      #pragma unroll
      for(int u=0;u<4;u++) bl_lds<NTA>(rq,L_Q+w*4096+u*1024,o+u*512,0); };
    __builtin_amdgcn_s_sleep(TOUCH_SLEEP);
    if(gv){
      gv_issue(0,l); gv_issue(1,l+4096); VMC(4); gv_issue(2,l+8192); gv_step(0,l); VMC(4); gv_issue(3,l); gv_step(1,l+4096);
      VMC(4); gv_step(2,l+8192); VMC(0); gv_step(3,l);
      MFMA_DRAIN();
      #pragma unroll
      for(int j=0;j<4;j++){ PIN(ac[j]); if(lane<16) ldsw<float>(L_RED+w*256+j*64+lane*4,ac[j][0]); }
      LGKM(0);
      unsigned old=0; if(lane==0) old=__hip_atomic_fetch_add((LAS unsigned*)(size_t)L_CNT,1u,__ATOMIC_RELAXED,__HIP_MEMORY_SCOPE_WORKGROUP);
      if(__builtin_amdgcn_readfirstlane(old)==3){
        if(lane<16){ const f32x4 s=add4(add4(ldsr<f32x4>(L_RED+lane*16),ldsr<f32x4>(L_RED+256+lane*16)),add4(ldsr<f32x4>(L_RED+512+lane*16),ldsr<f32x4>(L_RED+768+lane*16)));
          bs128<SC1>(tagged(s,tag),rf,OFF_MOD+b*256+lane*16,0); } }
    }
    STAMPH(25);
    asm volatile("":::"memory");
    q_issue();
    y_store();
    { u32x4 m0[5],m1[5]; const int t=w*64+lane; const unsigned o0=OFF_MOD+t*16, o1=o0+24576;
      #define M_LOADS(tail) asm volatile("buffer_load_dwordx4 %0, %10, %12, 0 offen sc1\n\tbuffer_load_dwordx4 %5, %11, %12, 0 offen sc1\n\tbuffer_load_dwordx4 %1, %10, %12, %13 offen sc1\n\tbuffer_load_dwordx4 %6, %11, %12, %13 offen sc1\n\t" \
                     "buffer_load_dwordx4 %2, %10, %12, %14 offen sc1\n\tbuffer_load_dwordx4 %7, %11, %12, %14 offen sc1\n\tbuffer_load_dwordx4 %3, %10, %12, %15 offen sc1\n\tbuffer_load_dwordx4 %8, %11, %12, %15 offen sc1\n\t" \
                     "buffer_load_dwordx4 %4, %10, %12, %16 offen sc1\n\tbuffer_load_dwordx4 %9, %11, %12, %16 offen sc1\n\t" tail \
          :"=&v"(m0[0]),"=&v"(m0[1]),"=&v"(m0[2]),"=&v"(m0[3]),"=&v"(m0[4]),"=&v"(m1[0]),"=&v"(m1[1]),"=&v"(m1[2]),"=&v"(m1[3]),"=&v"(m1[4]) \
          :"v"(o0),"v"(o1),"s"(rf),"s"(4096),"s"(8192),"s"(12288),"s"(16384):"memory")
      BAR_V(0);
      __builtin_amdgcn_s_sleep(M_SLEEP);
      const u64 t0=__builtin_amdgcn_s_memrealtime();
      { const int l=min(lane,39); poll1<255u>(rf,OFF_MOD+(l>=20? 24576:0)+((l%20)>>2)*4096+(w*4+(l&3))*256+252,tag); }
      for(;;){ M_LOADS("s_waitcnt vmcnt(0)"); unsigned m=0;
        #pragma unroll
        for(int j=0;j<5;j++) m=untag(untag(m,m0[j],tag),m1[j],tag);
        if(__all((m&255u)==0) || late(t0)) break; }
      #pragma unroll
      for(int j=0;j<5;j++){ const f32x4 m=__builtin_bit_cast(f32x4,m0[j]&0xffffff00u)+__builtin_bit_cast(f32x4,m1[j]&0xffffff00u); const unsigned p0=cvtpk(m[0],m[1]),p1=cvtpk(m[2],m[3]);
        f32x4 o={bflo(p0),bfhi(p0),bflo(p1),bfhi(p1)}; if(j==1||j==4) o+=1.f; ldsw<f32x4>(L_M+j*4096+t*16,o); } }
    STAMPH(24);
    BAR_L();
    if (wid<6){
      rsrc_t ra=mkrs(ws+OFF_H2,(ABL&4)?0:S_*D_*2), rb=mkrs(w1,(ABL&8)?0:D_*F_*2);
      const unsigned va=pm*32*8192+w*4096+lane*16; unsigned vb[7];
      #pragma unroll
      for(int i=0;i<7;i++){ int e=(7*w+i)*64+lane; int row=e/28, c=e-row*28; c^=((row>>3)&1)<<1; vb[i]=row*(F_*2)+c*16+pj*448; }
      auto issA=[&](int t,int slot){ const int kt=((t&~3)+((t+rot)&3)+xrot)&31;
        #pragma unroll
        for(int i=0;i<4;i++) bl_lds<SC1>(ra,slot*G1_SLOT+w*4096+i*1024,va+i*1024,kt*8192); };
      auto issB=[&](int t,int slot){ const int kt=((t&~3)+((t+rot)&3)+xrot)&31;
        #pragma unroll
        for(int i=0;i<7;i++) bl_lds<BAUX>(rb,slot*G1_SLOT+8192+(7*w+i)*1024,vb[i],kt*32*F_*2); };
      __builtin_amdgcn_s_sleep(PF_SLEEP);
      #pragma unroll 1
      for(int t=0;t<PF1;t++) issB(t,t);
      BAR(); BAR();
      __builtin_amdgcn_s_sleep(1);
      #pragma unroll 1
      for(int t=PF1;t<6;t++) issB(t,t);
      BAR();
      #pragma unroll 1
      for(int t=0;t<5;t++) issA(t,t);
      BAR_V(16); issA(5,5); BAR_V(16);
      issA(6,6); issB(6,6); BAR_V(23); issA(7,0); issB(7,0); BAR_V(30); issA(8,1); issB(8,1); BAR_V(37);
      int slot=2;
      #pragma unroll 1
      for(int s=4;s<G1_NT-5;s++){ issA(s+5,slot); issB(s+5,slot); slot=slot==G1_R-1?0:slot+1; BAR_V(44); }
      BAR_V(33); BAR_V(22); BAR_V(11); BAR_V(0); BAR();
    } else {
      BAR(); BAR();
      u32x4 xs[8];
      #pragma unroll
      for(int i=0;i<8;i++) RD128(xs[i],L_M+(w&1)*8192+lane*16,i*1024);
      BAR_L();
      auto touch=[&](int i){ touch_lines(mkrs(w2,D_*F_*2),(b*224+i*56+(w&1)*28+min(lane,27))*128); };
      #pragma unroll 1
      for(int s=1;s<=28;s++){ BAR();
        if(s==T0) touch(0); if(s==T1) touch(1); if(s==T2) touch(2); if(s==T3) touch(3); }
      { rsrc_t rx=mkrs(ws+OFF_X1,S_*D_*2); const int r_=pm*128+pj*8+(lane&7);
        const unsigned t_=(pm*32*8+((r_>>4)&7))*1024+(r_&15)*16+(lane>>5)*8192+((lane>>3)&3)*256+(w&1)*32768;
        #pragma unroll
        for(int i=0;i<8;i++){ PIN(xs[i]); bs128<SC1>(xs[i],rx,t_,(i>>2)*16384+(i&3)*65536); } }
      BAR(); BAR(); BAR(); BAR(); BAR();
      VMC(0); }
    { rsrc_t ra=mkrs(ws+OFF_HT,(ABL&1)?0:16*HT_PM), rb=mkrs(w2,(ABL&2)?0:D_*F_*2);
      const unsigned va=pm*HT_PM+w*2048+lane*16; unsigned vb[2];
      #pragma unroll
      for(int i=0;i<2;i++){ int e=(2*w+i)*64+lane; int row=e>>4,pos=e&15; int c=(((pos>>1)^(row&7))<<1)|(pos&1); vb[i]=row*2048+pn*256+c*16; }
      int tab;
      { const int p=lane+7;
        const int k=(p-7)/7, j=p-7-7*k, e=j+(j>=(pn&4)+((-li)&3));
        tab=kh*56+((e&4)|((e+li+lj)&3))*7+((0x3625140>>(4*min(k,6)))&7); }
      auto issA=[&](int kt,unsigned l){ bl_lds<SC1>(ra,l+w*2048,va,kt*8192); bl_lds<SC1>(ra,l+w*2048+1024,va+1024,kt*8192); };
      auto issB=[&](int kt,unsigned l){ bl_lds<BAUX>(rb,l+8192+w*2048,vb[0],kt*65536); bl_lds<BAUX>(rb,l+8192+w*2048+1024,vb[1],kt*65536); };
      #pragma unroll 1
      for(int u=0;u<7;u++) issB(kh*56+pn*7+u,u*16384);
      const int k7=__builtin_amdgcn_readlane(tab,0), k8=__builtin_amdgcn_readlane(tab,1);
      issB(k7,7*16384); issB(k8,8*16384);
      BAR_V(4);
      const unsigned f3=FL_3+(pm*64+kh*32+(lane&31))*128; unsigned fv;
      { u32x4 h0,h1; const unsigned a=3*16384+w*2048+lane*16; RD128(h0,a,0); RD128(h1,a,1024);
        asm volatile("buffer_load_dword %0, %3, %4, 0 offen sc1\n\ts_waitcnt lgkmcnt(0)":"=&v"(fv),"+v"(h0),"+v"(h1):"v"(f3),"s"(rf):"memory");
        const unsigned o=pm*HT_PM+(pj*7+3)*8192+w*2048+lane*16; bs128<SC1>(h0,ra,o,0); bs128<SC1>(h1,ra,o,1024); }
      asm volatile("s_barrier\n\ts_waitcnt vmcnt(2)":"+v"(fv)::"memory");
      if(__all(fv==tag)){ issA(k7,7*16384); issA(k8,8*16384); BAR(); }
      else { BAR(); poll1<~0u>(rf,f3,tag); issA(k7,7*16384); issA(k8,8*16384); }
      STAMPH(26);
      int p=9; unsigned l=0;
      auto iss=[&](){ const int kt=__builtin_amdgcn_readlane(tab,p-7); issA(kt,l); issB(kt,l); p++; l+=16384; if(l==G2_RING) l=0; };
      #pragma unroll 1
      for(int i=0;i<4;i++) iss();
      BAR_V(18);
      #pragma unroll 1
      for(int s=2;s<23;s++){
        if(s==5 && wid==4 && !(ABL&64)) poll1<~0u>(rf,f3+131072,tag);
        if(s==12 && wid==4 && !(ABL&64)) poll1<~0u>(rf,(f3+131072)^(32*128),tag);
        iss(); iss(); BAR_V(20); }
      iss();
      { const unsigned o=OFF_X1+(pm*32+pn*4+kh*2)*8192+w*4096+lane*16;
        #pragma unroll
        for(int i=0;i<4;i++) bl_lds<SC1>(rf,2*16384+w*4096+i*1024,o+i*1024,0);
        bl_lds<SC1>(rf,G2_RING+w*1024,OFF_MOD+(w&1)*24576+(5*1024+pn*128+kh*64)*4+min(lane,15)*16,0); }
      BAR_V(13); BAR_V(0);
      if(wid==4) STAMP_DUMP(24,32);
    }
    return;
  }
  int r8=lane&7, ub=w*8+(lane>>3), row=pm*128+pj*8+r8;
  auto stats_put=[&](const float* x,int slot){ f32x4 a={0,0,0,0},a2={0,0,0,0};
    #pragma unroll
    for(int i=0;i<8;i++){ const f32x4 v_={x[4*i],x[4*i+1],x[4*i+2],x[4*i+3]}; a+=v_; a2+=v_*v_; }
    const float s=xsum8_16_32((a[0]+a[1])+(a[2]+a[3])), s2=xsum8_16_32((a2[0]+a2[1])+(a2[2]+a2[3])); if(lane<8) ldsw<f32x2>(L_ST+slot*256+lane*32+w*8,(f32x2){s,s2}); };
  auto stats_get=[&](int slot,float& mu,float& rstd){ f32x4 a=ldsr<f32x4>(L_ST+slot*256+r8*32), c=ldsr<f32x4>(L_ST+slot*256+r8*32+16);
    mu=((a[0]+a[2])+(c[0]+c[2]))*(1.f/1024); const float var=fmaxf(((a[1]+a[3])+(c[1]+c[3]))*(1.f/1024)-mu*mu,0.f); rstd=__builtin_amdgcn_rsqf(var+1e-5f); };
  if((wid==0 && lane==0)) sig(rf,FL_0+(xcd*32+idx)*128,tag);
  unsigned uo[4], aB, aA0, aU;
  { const int r=lane&15, G=lane>>4;
    #pragma unroll
    for(int i=0;i<4;i++){ const int sb_=i*8+(lane>>3); uo[i]=OFF_UT+(sb_*1024+((ch+sb_*SKU)&1023))*128+(lane&7)*16; PIN(uo[i]); }
    aB=cb+CH_U+32+32*r+16*G-128; aA0=cb+((r&1)? CH_C0+2*(r+25-8*G) : CH_C1+2*(r+24-8*G)); aU=cb+CH_U+512+32*r+16*(G>>1)+8*(1-(G&1)); PIN(aB); PIN(aA0); PIN(aU); }
  if(wid==0 && F0_SLEEP) __builtin_amdgcn_s_sleep(F0_SLEEP);
  if(wid==0) poll1<~0u>(rf,FL_0+(xcd*32+(lane&31))*128,tag);
  BAR_L();
  STAMP(3);
  {
    #pragma unroll
    for(int i=0;i<4;i++) bl_lds<SC1>(rf,cb+CH_U+512+i*1024,uo[i],0);
    f32x32 cacc; u32x16 uu; int cnt;
    asm volatile("v_add_u32 v177, 0x400, v176\n\t"
      "v_add_u32 v178, 0x800, v176\n\t"
      "v_add_u32 v179, 0xc00, v176\n\t"
      "v_mov_b32 v16, 0\n\t"
      "v_mov_b32 v17, 0\n\t"
      "v_mov_b32 v18, 0\n\t"
      "v_mov_b32 v19, 0\n\t"
      "v_mov_b32 v20, 0\n\t"
      "v_mov_b32 v21, 0\n\t"
      "v_mov_b32 v22, 0\n\t"
      "v_mov_b32 v23, 0\n\t"
      "v_mov_b32 v24, 0\n\t"
      "v_mov_b32 v25, 0\n\t"
      "v_mov_b32 v26, 0\n\t"
      "v_mov_b32 v27, 0\n\t"
      "v_mov_b32 v28, 0\n\t"
      "v_mov_b32 v29, 0\n\t"
      "v_mov_b32 v30, 0\n\t"
      "v_mov_b32 v31, 0\n\t"
      "v_mov_b32 v32, 0\n\t"
      "v_mov_b32 v33, 0\n\t"
      "v_mov_b32 v34, 0\n\t"
      "v_mov_b32 v35, 0\n\t"
      "v_mov_b32 v36, 0\n\t"
      "v_mov_b32 v37, 0\n\t"
      "v_mov_b32 v38, 0\n\t"
      "v_mov_b32 v39, 0\n\t"
      "v_mov_b32 v40, 0\n\t"
      "v_mov_b32 v41, 0\n\t"
      "v_mov_b32 v42, 0\n\t"
      "v_mov_b32 v43, 0\n\t"
      "v_mov_b32 v44, 0\n\t"
      "v_mov_b32 v45, 0\n\t"
      "v_mov_b32 v46, 0\n\t"
      "v_mov_b32 v47, 0\n\t"
      "ds_read2_b32 v[48:49], v176 offset0:0 offset1:1\n\t"
      "ds_read2_b32 v[50:51], v176 offset0:2 offset1:3\n\t"
      "ds_read2_b32 v[52:53], v176 offset0:128 offset1:129\n\t"
      "ds_read2_b32 v[54:55], v176 offset0:130 offset1:131\n\t"
      "ds_read2_b32 v[56:57], v177 offset0:0 offset1:1\n\t"
      "ds_read2_b32 v[58:59], v177 offset0:2 offset1:3\n\t"
      "ds_read2_b32 v[60:61], v177 offset0:128 offset1:129\n\t"
      "ds_read2_b32 v[62:63], v177 offset0:130 offset1:131\n\t"
      "ds_read2_b32 v[64:65], v178 offset0:0 offset1:1\n\t"
      "ds_read2_b32 v[66:67], v178 offset0:2 offset1:3\n\t"
      "ds_read2_b32 v[68:69], v178 offset0:128 offset1:129\n\t"
      "ds_read2_b32 v[70:71], v178 offset0:130 offset1:131\n\t"
      "ds_read2_b32 v[72:73], v179 offset0:0 offset1:1\n\t"
      "ds_read2_b32 v[74:75], v179 offset0:2 offset1:3\n\t"
      "ds_read2_b32 v[76:77], v179 offset0:128 offset1:129\n\t"
      "ds_read2_b32 v[78:79], v179 offset0:130 offset1:131\n\t"
      "s_waitcnt vmcnt(0)\n\t"
      SA(4)
      "ds_read_b128 v[80:83], v180 offset:576\n\t"
      "ds_read_b128 v[84:87], v180 offset:1088\n\t"
      "ds_read_b128 v[88:91], v180 offset:1600\n\t"
      "ds_read_b128 v[92:95], v180 offset:2112\n\t"
      "ds_read_b128 v[96:99], v180 offset:2624\n\t"
      "ds_read_b128 v[100:103], v180 offset:3136\n\t"
      "ds_read_b128 v[104:107], v180 offset:3648\n\t"
      "ds_read_b128 v[108:111], v180 offset:4160\n\t"
      "ds_read_b64 v[182:183], v181 offset:0\n\t"
      "ds_read_b64 v[184:185], v181 offset:512\n\t"
      "ds_read_b64 v[186:187], v181 offset:1024\n\t"
      "ds_read_b64 v[188:189], v181 offset:1536\n\t"
      "ds_read_b64 v[190:191], v181 offset:2048\n\t"
      "ds_read_b64 v[192:193], v181 offset:2560\n\t"
      "ds_read_b64 v[194:195], v181 offset:3072\n\t"
      "ds_read_b64 v[196:197], v181 offset:3584\n\t"
      "s_mov_b32 %[n], 4\n\t"
      "s_waitcnt lgkmcnt(0)\n\t"
      ".p2align 6\n\t"
      "1:\n\t"
      "v_mfma_f32_16x16x32_bf16 v[16:19], v[48:51], v[80:83], v[16:19]\n\t"
      "ds_read2_b32 v[112:113], v176 offset0:16 offset1:17\n\t"
      "v_mfma_f32_16x16x32_bf16 v[20:23], v[48:51], v[84:87], v[20:23]\n\t"
      "ds_read2_b32 v[114:115], v176 offset0:18 offset1:19\n\t"
      "v_mfma_f32_16x16x32_bf16 v[24:27], v[48:51], v[88:91], v[24:27]\n\t"
      "ds_read_b128 v[144:147], v180 offset:512\n\t"
      "v_mfma_f32_16x16x32_bf16 v[28:31], v[48:51], v[92:95], v[28:31]\n\t"
      "ds_read2_b32 v[116:117], v176 offset0:144 offset1:145\n\t"
      "v_mfma_f32_16x16x32_bf16 v[32:35], v[48:51], v[96:99], v[32:35]\n\t"
      "ds_read2_b32 v[118:119], v176 offset0:146 offset1:147\n\t"
      "v_mfma_f32_16x16x32_bf16 v[36:39], v[48:51], v[100:103], v[36:39]\n\t"
      "ds_read_b128 v[148:151], v180 offset:1024\n\t"
      "v_mfma_f32_16x16x32_bf16 v[40:43], v[48:51], v[104:107], v[40:43]\n\t"
      "ds_read2_b32 v[120:121], v177 offset0:16 offset1:17\n\t"
      "v_mfma_f32_16x16x32_bf16 v[44:47], v[48:51], v[108:111], v[44:47]\n\t"
      "ds_read2_b32 v[122:123], v177 offset0:18 offset1:19\n\t"
      "v_mfma_f32_16x16x32_bf16 v[44:47], v[52:55], v[104:107], v[44:47]\n\t"
      "ds_read_b128 v[152:155], v180 offset:1536\n\t"
      "v_mfma_f32_16x16x32_bf16 v[40:43], v[52:55], v[100:103], v[40:43]\n\t"
      "ds_read2_b32 v[124:125], v177 offset0:144 offset1:145\n\t"
      "v_mfma_f32_16x16x32_bf16 v[36:39], v[52:55], v[96:99], v[36:39]\n\t"
      "ds_read2_b32 v[126:127], v177 offset0:146 offset1:147\n\t"
      "v_mfma_f32_16x16x32_bf16 v[32:35], v[52:55], v[92:95], v[32:35]\n\t"
      "ds_read_b128 v[156:159], v180 offset:2048\n\t"
      "v_mfma_f32_16x16x32_bf16 v[28:31], v[52:55], v[88:91], v[28:31]\n\t"
      "ds_read2_b32 v[128:129], v178 offset0:16 offset1:17\n\t"
      "v_mfma_f32_16x16x32_bf16 v[24:27], v[52:55], v[84:87], v[24:27]\n\t"
      "ds_read2_b32 v[130:131], v178 offset0:18 offset1:19\n\t"
      "v_mfma_f32_16x16x32_bf16 v[20:23], v[52:55], v[80:83], v[20:23]\n\t"
      "ds_read_b128 v[160:163], v180 offset:2560\n\t"
      "v_mfma_f32_16x16x32_bf16 v[24:27], v[56:59], v[80:83], v[24:27]\n\t"
      "ds_read2_b32 v[132:133], v178 offset0:144 offset1:145\n\t"
      "v_mfma_f32_16x16x32_bf16 v[28:31], v[56:59], v[84:87], v[28:31]\n\t"
      "ds_read2_b32 v[134:135], v178 offset0:146 offset1:147\n\t"
      "v_mfma_f32_16x16x32_bf16 v[32:35], v[56:59], v[88:91], v[32:35]\n\t"
      "ds_read_b128 v[164:167], v180 offset:3072\n\t"
      "v_mfma_f32_16x16x32_bf16 v[36:39], v[56:59], v[92:95], v[36:39]\n\t"
      "ds_read2_b32 v[136:137], v179 offset0:16 offset1:17\n\t"
      "v_mfma_f32_16x16x32_bf16 v[40:43], v[56:59], v[96:99], v[40:43]\n\t"
      "ds_read2_b32 v[138:139], v179 offset0:18 offset1:19\n\t"
      "v_mfma_f32_16x16x32_bf16 v[44:47], v[56:59], v[100:103], v[44:47]\n\t"
      "ds_read_b128 v[168:171], v180 offset:3584\n\t"
      "v_mfma_f32_16x16x32_bf16 v[44:47], v[60:63], v[96:99], v[44:47]\n\t"
      "ds_read2_b32 v[140:141], v179 offset0:144 offset1:145\n\t"
      "v_mfma_f32_16x16x32_bf16 v[40:43], v[60:63], v[92:95], v[40:43]\n\t"
      "ds_read2_b32 v[142:143], v179 offset0:146 offset1:147\n\t"
      "v_mfma_f32_16x16x32_bf16 v[36:39], v[60:63], v[88:91], v[36:39]\n\t"
      "ds_read_b128 v[172:175], v180 offset:4096\n\t"
      "v_mfma_f32_16x16x32_bf16 v[32:35], v[60:63], v[84:87], v[32:35]\n\t"
      "v_mfma_f32_16x16x32_bf16 v[28:31], v[60:63], v[80:83], v[28:31]\n\t"
      "v_mfma_f32_16x16x32_bf16 v[32:35], v[64:67], v[80:83], v[32:35]\n\t"
      "v_mfma_f32_16x16x32_bf16 v[36:39], v[64:67], v[84:87], v[36:39]\n\t"
      "v_mfma_f32_16x16x32_bf16 v[40:43], v[64:67], v[88:91], v[40:43]\n\t"
      "v_mfma_f32_16x16x32_bf16 v[44:47], v[64:67], v[92:95], v[44:47]\n\t"
      "v_mfma_f32_16x16x32_bf16 v[44:47], v[68:71], v[88:91], v[44:47]\n\t"
      "v_mfma_f32_16x16x32_bf16 v[40:43], v[68:71], v[84:87], v[40:43]\n\t"
      "v_mfma_f32_16x16x32_bf16 v[36:39], v[68:71], v[80:83], v[36:39]\n\t"
      "v_mfma_f32_16x16x32_bf16 v[40:43], v[72:75], v[80:83], v[40:43]\n\t"
      "v_mfma_f32_16x16x32_bf16 v[44:47], v[72:75], v[84:87], v[44:47]\n\t"
      "s_waitcnt lgkmcnt(0)\n\t"
      "v_mfma_f32_16x16x32_bf16 v[44:47], v[76:79], v[80:83], v[44:47]\n\t"
      "v_mfma_f32_16x16x32_bf16 v[16:19], v[112:115], v[144:147], v[16:19]\n\t"
      "ds_read2_b32 v[48:49], v176 offset0:32 offset1:33\n\t"
      "v_mfma_f32_16x16x32_bf16 v[20:23], v[112:115], v[148:151], v[20:23]\n\t"
      "ds_read2_b32 v[50:51], v176 offset0:34 offset1:35\n\t"
      "v_mfma_f32_16x16x32_bf16 v[24:27], v[112:115], v[152:155], v[24:27]\n\t"
      "ds_read_b128 v[80:83], v180 offset:448\n\t"
      "v_mfma_f32_16x16x32_bf16 v[28:31], v[112:115], v[156:159], v[28:31]\n\t"
      "ds_read2_b32 v[52:53], v176 offset0:160 offset1:161\n\t"
      "v_mfma_f32_16x16x32_bf16 v[32:35], v[112:115], v[160:163], v[32:35]\n\t"
      "ds_read2_b32 v[54:55], v176 offset0:162 offset1:163\n\t"
      "v_mfma_f32_16x16x32_bf16 v[36:39], v[112:115], v[164:167], v[36:39]\n\t"
      "ds_read_b128 v[84:87], v180 offset:960\n\t"
      "v_mfma_f32_16x16x32_bf16 v[40:43], v[112:115], v[168:171], v[40:43]\n\t"
      "ds_read2_b32 v[56:57], v177 offset0:32 offset1:33\n\t"
      "v_mfma_f32_16x16x32_bf16 v[44:47], v[112:115], v[172:175], v[44:47]\n\t"
      "ds_read2_b32 v[58:59], v177 offset0:34 offset1:35\n\t"
      "v_mfma_f32_16x16x32_bf16 v[44:47], v[116:119], v[168:171], v[44:47]\n\t"
      "ds_read_b128 v[88:91], v180 offset:1472\n\t"
      "v_mfma_f32_16x16x32_bf16 v[40:43], v[116:119], v[164:167], v[40:43]\n\t"
      "ds_read2_b32 v[60:61], v177 offset0:160 offset1:161\n\t"
      "v_mfma_f32_16x16x32_bf16 v[36:39], v[116:119], v[160:163], v[36:39]\n\t"
      "ds_read2_b32 v[62:63], v177 offset0:162 offset1:163\n\t"
      "v_mfma_f32_16x16x32_bf16 v[32:35], v[116:119], v[156:159], v[32:35]\n\t"
      "ds_read_b128 v[92:95], v180 offset:1984\n\t"
      "v_mfma_f32_16x16x32_bf16 v[28:31], v[116:119], v[152:155], v[28:31]\n\t"
      "ds_read2_b32 v[64:65], v178 offset0:32 offset1:33\n\t"
      "v_mfma_f32_16x16x32_bf16 v[24:27], v[116:119], v[148:151], v[24:27]\n\t"
      "ds_read2_b32 v[66:67], v178 offset0:34 offset1:35\n\t"
      "v_mfma_f32_16x16x32_bf16 v[20:23], v[116:119], v[144:147], v[20:23]\n\t"
      "ds_read_b128 v[96:99], v180 offset:2496\n\t"
      "v_mfma_f32_16x16x32_bf16 v[24:27], v[120:123], v[144:147], v[24:27]\n\t"
      "ds_read2_b32 v[68:69], v178 offset0:160 offset1:161\n\t"
      "v_mfma_f32_16x16x32_bf16 v[28:31], v[120:123], v[148:151], v[28:31]\n\t"
      "ds_read2_b32 v[70:71], v178 offset0:162 offset1:163\n\t"
      "v_mfma_f32_16x16x32_bf16 v[32:35], v[120:123], v[152:155], v[32:35]\n\t"
      "ds_read_b128 v[100:103], v180 offset:3008\n\t"
      "v_mfma_f32_16x16x32_bf16 v[36:39], v[120:123], v[156:159], v[36:39]\n\t"
      "ds_read2_b32 v[72:73], v179 offset0:32 offset1:33\n\t"
      "v_mfma_f32_16x16x32_bf16 v[40:43], v[120:123], v[160:163], v[40:43]\n\t"
      "ds_read2_b32 v[74:75], v179 offset0:34 offset1:35\n\t"
      "v_mfma_f32_16x16x32_bf16 v[44:47], v[120:123], v[164:167], v[44:47]\n\t"
      "ds_read_b128 v[104:107], v180 offset:3520\n\t"
      "v_mfma_f32_16x16x32_bf16 v[44:47], v[124:127], v[160:163], v[44:47]\n\t"
      "ds_read2_b32 v[76:77], v179 offset0:160 offset1:161\n\t"
      "v_mfma_f32_16x16x32_bf16 v[40:43], v[124:127], v[156:159], v[40:43]\n\t"
      "ds_read2_b32 v[78:79], v179 offset0:162 offset1:163\n\t"
      "v_mfma_f32_16x16x32_bf16 v[36:39], v[124:127], v[152:155], v[36:39]\n\t"
      "ds_read_b128 v[108:111], v180 offset:4032\n\t"
      "v_mfma_f32_16x16x32_bf16 v[32:35], v[124:127], v[148:151], v[32:35]\n\t"
      "v_add_u32 v176, 0x80, v176\n\t"
      "v_mfma_f32_16x16x32_bf16 v[28:31], v[124:127], v[144:147], v[28:31]\n\t"
      "v_add_u32 v177, 0x80, v177\n\t"
      "v_mfma_f32_16x16x32_bf16 v[32:35], v[128:131], v[144:147], v[32:35]\n\t"
      "v_add_u32 v178, 0x80, v178\n\t"
      "v_mfma_f32_16x16x32_bf16 v[36:39], v[128:131], v[148:151], v[36:39]\n\t"
      "v_add_u32 v179, 0x80, v179\n\t"
      "v_mfma_f32_16x16x32_bf16 v[40:43], v[128:131], v[152:155], v[40:43]\n\t"
      "v_subrev_u32 v180, 0x80, v180\n\t"
      "v_mfma_f32_16x16x32_bf16 v[44:47], v[128:131], v[156:159], v[44:47]\n\t"
      "s_add_i32 %[n], %[n], -1\n\t"
      "v_mfma_f32_16x16x32_bf16 v[44:47], v[132:135], v[152:155], v[44:47]\n\t"
      "s_cmp_lg_u32 %[n], 0\n\t"
      "v_mfma_f32_16x16x32_bf16 v[40:43], v[132:135], v[148:151], v[40:43]\n\t"
      "v_mfma_f32_16x16x32_bf16 v[36:39], v[132:135], v[144:147], v[36:39]\n\t"
      "v_mfma_f32_16x16x32_bf16 v[40:43], v[136:139], v[144:147], v[40:43]\n\t"
      "v_mfma_f32_16x16x32_bf16 v[44:47], v[136:139], v[148:151], v[44:47]\n\t"
      "s_waitcnt lgkmcnt(0)\n\t"
      "v_mfma_f32_16x16x32_bf16 v[44:47], v[140:143], v[144:147], v[44:47]\n\t"
      "s_cbranch_scc1 1b\n\t"
      "s_nop 7\n\t"
      "s_nop 7\n\t"
      : "={v[16:47]}"(cacc), "={v[182:197]}"(uu), "+{v176}"(aA0), "+{v180}"(aB), [n]"=&s"(cnt) SA_OPS : "{v181}"(aU) : "memory","scc" SA_CLOB,"v48","v49","v50","v51","v52","v53","v54","v55","v56","v57","v58","v59","v60","v61","v62","v63","v64","v65","v66","v67","v68","v69","v70","v71","v72","v73","v74","v75","v76","v77","v78","v79","v80","v81","v82","v83","v84","v85","v86","v87","v88","v89","v90","v91","v92","v93","v94","v95","v96","v97","v98","v99","v100","v101","v102","v103","v104","v105","v106","v107","v108","v109","v110","v111","v112","v113","v114","v115","v116","v117","v118","v119","v120","v121","v122","v123","v124","v125","v126","v127","v128","v129","v130","v131","v132","v133","v134","v135","v136","v137","v138","v139","v140","v141","v142","v143","v144","v145","v146","v147","v148","v149","v150","v151","v152","v153","v154","v155","v156","v157","v158","v159","v160","v161","v162","v163","v164","v165","v166","v167","v168","v169","v170","v171","v172","v173","v174","v175","v177","v178","v179");
    lane=lane_id(); r8=lane&7; ub=w*8+(lane>>3); row=pm*128+pj*8+r8; const int r=lane&15, G=lane>>4;
    const float Dv=bflo(dv);
    #pragma unroll
    for(int n=0;n<8;n++){ const f32x4 y=q4of(cacc,n);
      ldsw<u32x2>(cb+CH_C0+512*n+32*r+8*G,(u32x2){cvtpk(y[0]+bfhi(uu[2*n+1])*Dv,y[1]+bflo(uu[2*n+1])*Dv),cvtpk(y[2]+bfhi(uu[2*n])*Dv,y[3]+bflo(uu[2*n])*Dv)}); }
  }
  STAMP(5);
  y_store();
  float x[32];
  #pragma unroll
  for(int u=0;u<4;u++) unpack8(ldsr<u32x4>(L_Q+w*4096+u*1024+lane*16),x+8*u);
  stats_put(x,0);
  BAR_VL(0);
  STAMP(6);
  if(wid==0 && lane<8) sig(rf,FL_1+lane*8192+(b>>2)*128+(b&3)*4,tag);
  u32x4 xo[4]; const unsigned tb=(pm*32*8+((row>>4)&7))*1024+(row&15)*16+(ub>>2)*8192+(ub&3)*256;
  { float mu,rstd; stats_get(0,mu,rstd);
    f32x4 gq[8]; const float nb1=-mu*rstd;
    #pragma unroll
    for(int i=0;i<8;i++){ gq[i]=(f32x4){x[4*i],x[4*i+1],x[4*i+2],x[4*i+3]}*rstd+nb1; PIN(gq[i]); }
    unsigned yo[8], am=L_M+ub*32;
    #pragma unroll
    for(int i=0;i<8;i++){ yo[i]=OFF_YX+((row>>4)*256+((2*ub+64*(i>>1)+(i&1)+(row>>4)*SKY)&255))*128+(row&15)*8; PIN(yo[i]); }
    PIN(am);
    if(wid==0){ if(F1_SLEEP) __builtin_amdgcn_s_sleep(F1_SLEEP); poll4(rf,FL_1+xcd*8192+lane*128,tag); if(b==0 && lane==0) sig(rf,FL_E,tag); }
    STAMP(22);
    BAR();
    STAMP(7);
    u32x2 yv[4][2];
    #pragma unroll
    for(int i=0;i<8;i++) BLOAD("buffer_load_dwordx2",yv[i>>1][i&1],yo[i],0);
    { f32x4 m[3][8];
      #pragma unroll
      for(int u=0;u<8;u++)
        #pragma unroll
        for(int j=0;j<3;j++) RD128(m[j][u],am,j*4096+(u>>1)*1024+(u&1)*16);
      #pragma unroll
      for(int h=0;h<2;h++){
        asm volatile("s_waitcnt lgkmcnt(%12)":"+v"(m[0][4*h]),"+v"(m[1][4*h]),"+v"(m[2][4*h]),"+v"(m[0][4*h+1]),"+v"(m[1][4*h+1]),"+v"(m[2][4*h+1]),"+v"(m[0][4*h+2]),"+v"(m[1][4*h+2]),"+v"(m[2][4*h+2]),"+v"(m[0][4*h+3]),"+v"(m[1][4*h+3]),"+v"(m[2][4*h+3]):"i"(12-12*h):"memory");
        #pragma unroll
        for(int u=4*h;u<4*h+4;u++) gq[u]=m[2][u]*(gq[u]*m[1][u]+m[0][u]); } }
    f32x4 sb[8],cb_[8];
    #pragma unroll
    for(int u=0;u<8;u++){ PIN(gq[u]); RD128(sb[u],am,12288+(u>>1)*1024+(u&1)*16); RD128(cb_[u],am,16384+(u>>1)*1024+(u&1)*16); }
    asm volatile("s_waitcnt vmcnt(0)":"+v"(yv[0][0]),"+v"(yv[0][1]),"+v"(yv[1][0]),"+v"(yv[1][1]),"+v"(yv[2][0]),"+v"(yv[2][1]),"+v"(yv[3][0]),"+v"(yv[3][1])::"memory");
    rsrc_t rh=mkrs(ws+OFF_H2,S_*D_*2);
    #pragma unroll
    for(int u=0;u<4;u++){ float y[8]; unpack8(cat(yv[u][0],yv[u][1]),y);
      #pragma unroll
      for(int e=0;e<8;e++) x[8*u+e]+=gq[2*u+(e>>2)][e&3]*y[e];
      xo[u]=pack8(x+8*u); unpack8(xo[u],x+8*u); }
    stats_put(x,1);
    asm volatile("s_waitcnt lgkmcnt(0)\n\ts_barrier":"+v"(sb[0]),"+v"(sb[1]),"+v"(sb[2]),"+v"(sb[3]),"+v"(sb[4]),"+v"(sb[5]),"+v"(sb[6]),"+v"(sb[7]),
      "+v"(cb_[0]),"+v"(cb_[1]),"+v"(cb_[2]),"+v"(cb_[3]),"+v"(cb_[4]),"+v"(cb_[5]),"+v"(cb_[6]),"+v"(cb_[7])::"memory");
    stats_get(1,mu,rstd); const float nb=-mu*rstd;
    #pragma unroll
    for(int u=0;u<4;u++){ float h[8];
      #pragma unroll
      for(int e=0;e<8;e++) h[e]=(x[8*u+e]*rstd+nb)*cb_[2*u+(e>>2)][e&3]+sb[2*u+(e>>2)][e&3];
      bs128<SC1>(pack8(h),rh,tb,u*65536); }
    #pragma unroll
    for(int u=0;u<4;u++) ldsw<u32x4>(L_M+w*4096+u*1024+lane*16,xo[u]);
  }
  STAMP(8);
  BAR_VL(0);
  STAMP(9);
  if(wid==0){ if(lane==0) sig(rf,FL_2+(pm*16+pj)*128,tag); if(F2_SLEEP) __builtin_amdgcn_s_sleep(F2_SLEEP); poll1<~0u>(rf,FL_2+(pm*16+(lane&15))*128,tag); }
  BAR();
  STAMP(10);
  const int wm=wid>>1, wn=wid&1; int G=lane>>4, q4=(lane>>2)&3, p4=lane&3;
  unsigned aX=wm*4096+lane*16, aW[4];
  #pragma unroll
  for(int j=0;j<4;j++){ const int kk=4*G+q4, jt=(((j>>1)^kh)*4+2*wn+(j&1)); aW[j]=8192+kk*256+((jt^(kk&7))*32)+8*p4; PIN(aW[j]); }
  PIN(aX);
  { const int sw=(G&1)*32; const unsigned kp=8192+(8*G+q4)*448+8*p4;
    const unsigned aA=aX, aE=kp+256*wn+sw, aO=kp+256*wn-sw, aS=kp+(wn? 224-sw : 192+sw);
    f32x32 h0,h1,h2_; f32x16 h3; unsigned so; int cnt;
    asm volatile("v_mov_b32 v16, 0\n\t"
      "v_mov_b32 v17, 0\n\t"
      "v_mov_b32 v18, 0\n\t"
      "v_mov_b32 v19, 0\n\t"
      "v_mov_b32 v20, 0\n\t"
      "v_mov_b32 v21, 0\n\t"
      "v_mov_b32 v22, 0\n\t"
      "v_mov_b32 v23, 0\n\t"
      "v_mov_b32 v24, 0\n\t"
      "v_mov_b32 v25, 0\n\t"
      "v_mov_b32 v26, 0\n\t"
      "v_mov_b32 v27, 0\n\t"
      "v_mov_b32 v28, 0\n\t"
      "v_mov_b32 v29, 0\n\t"
      "v_mov_b32 v30, 0\n\t"
      "v_mov_b32 v31, 0\n\t"
      "v_mov_b32 v32, 0\n\t"
      "v_mov_b32 v33, 0\n\t"
      "v_mov_b32 v34, 0\n\t"
      "v_mov_b32 v35, 0\n\t"
      "v_mov_b32 v36, 0\n\t"
      "v_mov_b32 v37, 0\n\t"
      "v_mov_b32 v38, 0\n\t"
      "v_mov_b32 v39, 0\n\t"
      "v_mov_b32 v40, 0\n\t"
      "v_mov_b32 v41, 0\n\t"
      "v_mov_b32 v42, 0\n\t"
      "v_mov_b32 v43, 0\n\t"
      "v_mov_b32 v44, 0\n\t"
      "v_mov_b32 v45, 0\n\t"
      "v_mov_b32 v46, 0\n\t"
      "v_mov_b32 v47, 0\n\t"
      "v_mov_b32 v48, 0\n\t"
      "v_mov_b32 v49, 0\n\t"
      "v_mov_b32 v50, 0\n\t"
      "v_mov_b32 v51, 0\n\t"
      "v_mov_b32 v52, 0\n\t"
      "v_mov_b32 v53, 0\n\t"
      "v_mov_b32 v54, 0\n\t"
      "v_mov_b32 v55, 0\n\t"
      "v_mov_b32 v56, 0\n\t"
      "v_mov_b32 v57, 0\n\t"
      "v_mov_b32 v58, 0\n\t"
      "v_mov_b32 v59, 0\n\t"
      "v_mov_b32 v60, 0\n\t"
      "v_mov_b32 v61, 0\n\t"
      "v_mov_b32 v62, 0\n\t"
      "v_mov_b32 v63, 0\n\t"
      "v_mov_b32 v64, 0\n\t"
      "v_mov_b32 v65, 0\n\t"
      "v_mov_b32 v66, 0\n\t"
      "v_mov_b32 v67, 0\n\t"
      "v_mov_b32 v68, 0\n\t"
      "v_mov_b32 v69, 0\n\t"
      "v_mov_b32 v70, 0\n\t"
      "v_mov_b32 v71, 0\n\t"
      "v_mov_b32 v72, 0\n\t"
      "v_mov_b32 v73, 0\n\t"
      "v_mov_b32 v74, 0\n\t"
      "v_mov_b32 v75, 0\n\t"
      "v_mov_b32 v76, 0\n\t"
      "v_mov_b32 v77, 0\n\t"
      "v_mov_b32 v78, 0\n\t"
      "v_mov_b32 v79, 0\n\t"
      "v_mov_b32 v80, 0\n\t"
      "v_mov_b32 v81, 0\n\t"
      "v_mov_b32 v82, 0\n\t"
      "v_mov_b32 v83, 0\n\t"
      "v_mov_b32 v84, 0\n\t"
      "v_mov_b32 v85, 0\n\t"
      "v_mov_b32 v86, 0\n\t"
      "v_mov_b32 v87, 0\n\t"
      "v_mov_b32 v88, 0\n\t"
      "v_mov_b32 v89, 0\n\t"
      "v_mov_b32 v90, 0\n\t"
      "v_mov_b32 v91, 0\n\t"
      "v_mov_b32 v92, 0\n\t"
      "v_mov_b32 v93, 0\n\t"
      "v_mov_b32 v94, 0\n\t"
      "v_mov_b32 v95, 0\n\t"
      "v_mov_b32 v96, 0\n\t"
      "v_mov_b32 v97, 0\n\t"
      "v_mov_b32 v98, 0\n\t"
      "v_mov_b32 v99, 0\n\t"
      "v_mov_b32 v100, 0\n\t"
      "v_mov_b32 v101, 0\n\t"
      "v_mov_b32 v102, 0\n\t"
      "v_mov_b32 v103, 0\n\t"
      "v_mov_b32 v104, 0\n\t"
      "v_mov_b32 v105, 0\n\t"
      "v_mov_b32 v106, 0\n\t"
      "v_mov_b32 v107, 0\n\t"
      "v_mov_b32 v108, 0\n\t"
      "v_mov_b32 v109, 0\n\t"
      "v_mov_b32 v110, 0\n\t"
      "v_mov_b32 v111, 0\n\t"
      "v_mov_b32 v112, 0\n\t"
      "v_mov_b32 v113, 0\n\t"
      "v_mov_b32 v114, 0\n\t"
      "v_mov_b32 v115, 0\n\t"
      "v_mov_b32 v116, 0\n\t"
      "v_mov_b32 v117, 0\n\t"
      "v_mov_b32 v118, 0\n\t"
      "v_mov_b32 v119, 0\n\t"
      "v_mov_b32 v120, 0\n\t"
      "v_mov_b32 v121, 0\n\t"
      "v_mov_b32 v122, 0\n\t"
      "v_mov_b32 v123, 0\n\t"
      "v_mov_b32 v124, 0\n\t"
      "v_mov_b32 v125, 0\n\t"
      "v_mov_b32 v126, 0\n\t"
      "v_mov_b32 v127, 0\n\t"
      "s_mov_b32 %[so], %[st]\n\t"
      "s_mov_b32 %[n], 16\n\t"
      "v_add_u32 v220, %[so], v216\n\t"
      "v_add_u32 v221, %[so], v217\n\t"
      "v_add_u32 v222, %[so], v218\n\t"
      "v_add_u32 v223, %[so], v219\n\t"
      "s_barrier\n\t"
      SA(11)
      "ds_read_b128 v[128:131], v216 offset:0\n\t"
      "ds_read_b64_tr_b16 v[144:145], v217 offset:0\n\t"
      "ds_read_b64_tr_b16 v[146:147], v217 offset:1792\n\t"
      "ds_read_b128 v[132:135], v216 offset:1024\n\t"
      "ds_read_b128 v[136:139], v216 offset:2048\n\t"
      "ds_read_b128 v[140:143], v216 offset:3072\n\t"
      "ds_read_b64_tr_b16 v[148:149], v218 offset:32\n\t"
      "ds_read_b64_tr_b16 v[150:151], v218 offset:1824\n\t"
      "ds_read_b64_tr_b16 v[152:153], v217 offset:64\n\t"
      "ds_read_b64_tr_b16 v[154:155], v217 offset:1856\n\t"
      "ds_read_b64_tr_b16 v[156:157], v218 offset:96\n\t"
      "ds_read_b64_tr_b16 v[158:159], v218 offset:1888\n\t"
      "ds_read_b64_tr_b16 v[160:161], v217 offset:128\n\t"
      "ds_read_b64_tr_b16 v[162:163], v217 offset:1920\n\t"
      "ds_read_b64_tr_b16 v[164:165], v218 offset:160\n\t"
      "ds_read_b64_tr_b16 v[166:167], v218 offset:1952\n\t"
      "ds_read_b64_tr_b16 v[168:169], v219 offset:0\n\t"
      "ds_read_b64_tr_b16 v[170:171], v219 offset:1792\n\t"
      "s_waitcnt lgkmcnt(0)\n\t"
      ".p2align 6\n\t"
      "1:\n\t"
      "s_barrier\n\t"
      "v_mfma_f32_16x16x32_bf16 v[16:19], v[144:147], v[128:131], v[16:19]\n\t"
      "ds_read_b128 v[172:175], v220 offset:0\n\t"
      "s_add_i32 %[so], %[so], %[st]\n\t"
      "v_mfma_f32_16x16x32_bf16 v[20:23], v[144:147], v[132:135], v[20:23]\n\t"
      "ds_read_b64_tr_b16 v[188:189], v221 offset:0\n\t"
      "s_cmp_lg_u32 %[so], %[lim]\n\t"
      "v_mfma_f32_16x16x32_bf16 v[24:27], v[144:147], v[136:139], v[24:27]\n\t"
      "ds_read_b64_tr_b16 v[190:191], v221 offset:1792\n\t"
      "s_cselect_b32 %[so], %[so], 0\n\t"
      "v_mfma_f32_16x16x32_bf16 v[28:31], v[144:147], v[140:143], v[28:31]\n\t"
      "ds_read_b128 v[176:179], v220 offset:1024\n\t"
      "v_mfma_f32_16x16x32_bf16 v[44:47], v[148:151], v[140:143], v[44:47]\n\t"
      "ds_read_b128 v[180:183], v220 offset:2048\n\t"
      "v_mfma_f32_16x16x32_bf16 v[40:43], v[148:151], v[136:139], v[40:43]\n\t"
      "ds_read_b128 v[184:187], v220 offset:3072\n\t"
      "v_mfma_f32_16x16x32_bf16 v[36:39], v[148:151], v[132:135], v[36:39]\n\t"
      "ds_read_b64_tr_b16 v[192:193], v222 offset:32\n\t"
      "v_mfma_f32_16x16x32_bf16 v[32:35], v[148:151], v[128:131], v[32:35]\n\t"
      "ds_read_b64_tr_b16 v[194:195], v222 offset:1824\n\t"
      "v_mfma_f32_16x16x32_bf16 v[48:51], v[152:155], v[128:131], v[48:51]\n\t"
      "ds_read_b64_tr_b16 v[196:197], v221 offset:64\n\t"
      "v_mfma_f32_16x16x32_bf16 v[52:55], v[152:155], v[132:135], v[52:55]\n\t"
      "ds_read_b64_tr_b16 v[198:199], v221 offset:1856\n\t"
      "v_mfma_f32_16x16x32_bf16 v[56:59], v[152:155], v[136:139], v[56:59]\n\t"
      "ds_read_b64_tr_b16 v[200:201], v222 offset:96\n\t"
      "v_mfma_f32_16x16x32_bf16 v[60:63], v[152:155], v[140:143], v[60:63]\n\t"
      "ds_read_b64_tr_b16 v[202:203], v222 offset:1888\n\t"
      "v_mfma_f32_16x16x32_bf16 v[76:79], v[156:159], v[140:143], v[76:79]\n\t"
      "ds_read_b64_tr_b16 v[204:205], v221 offset:128\n\t"
      "v_mfma_f32_16x16x32_bf16 v[72:75], v[156:159], v[136:139], v[72:75]\n\t"
      "ds_read_b64_tr_b16 v[206:207], v221 offset:1920\n\t"
      "v_mfma_f32_16x16x32_bf16 v[68:71], v[156:159], v[132:135], v[68:71]\n\t"
      "ds_read_b64_tr_b16 v[208:209], v222 offset:160\n\t"
      "v_mfma_f32_16x16x32_bf16 v[64:67], v[156:159], v[128:131], v[64:67]\n\t"
      "ds_read_b64_tr_b16 v[210:211], v222 offset:1952\n\t"
      "v_mfma_f32_16x16x32_bf16 v[80:83], v[160:163], v[128:131], v[80:83]\n\t"
      "ds_read_b64_tr_b16 v[212:213], v223 offset:0\n\t"
      "v_mfma_f32_16x16x32_bf16 v[84:87], v[160:163], v[132:135], v[84:87]\n\t"
      "ds_read_b64_tr_b16 v[214:215], v223 offset:1792\n\t"
      "v_mfma_f32_16x16x32_bf16 v[88:91], v[160:163], v[136:139], v[88:91]\n\t"
      "v_add_u32 v220, %[so], v216\n\t"
      "v_add_u32 v221, %[so], v217\n\t"
      "v_mfma_f32_16x16x32_bf16 v[92:95], v[160:163], v[140:143], v[92:95]\n\t"
      "v_add_u32 v222, %[so], v218\n\t"
      "v_add_u32 v223, %[so], v219\n\t"
      "v_mfma_f32_16x16x32_bf16 v[108:111], v[164:167], v[140:143], v[108:111]\n\t"
      "v_mfma_f32_16x16x32_bf16 v[104:107], v[164:167], v[136:139], v[104:107]\n\t"
      "v_mfma_f32_16x16x32_bf16 v[100:103], v[164:167], v[132:135], v[100:103]\n\t"
      "v_mfma_f32_16x16x32_bf16 v[96:99], v[164:167], v[128:131], v[96:99]\n\t"
      "v_mfma_f32_16x16x32_bf16 v[112:115], v[168:171], v[128:131], v[112:115]\n\t"
      "v_mfma_f32_16x16x32_bf16 v[116:119], v[168:171], v[132:135], v[116:119]\n\t"
      "v_mfma_f32_16x16x32_bf16 v[120:123], v[168:171], v[136:139], v[120:123]\n\t"
      "s_waitcnt lgkmcnt(0)\n\t"
      "v_mfma_f32_16x16x32_bf16 v[124:127], v[168:171], v[140:143], v[124:127]\n\t"
      "s_barrier\n\t"
      "v_mfma_f32_16x16x32_bf16 v[16:19], v[188:191], v[172:175], v[16:19]\n\t"
      "ds_read_b128 v[128:131], v220 offset:0\n\t"
      "s_add_i32 %[so], %[so], %[st]\n\t"
      "v_mfma_f32_16x16x32_bf16 v[20:23], v[188:191], v[176:179], v[20:23]\n\t"
      "ds_read_b64_tr_b16 v[144:145], v221 offset:0\n\t"
      "s_cmp_lg_u32 %[so], %[lim]\n\t"
      "v_mfma_f32_16x16x32_bf16 v[24:27], v[188:191], v[180:183], v[24:27]\n\t"
      "ds_read_b64_tr_b16 v[146:147], v221 offset:1792\n\t"
      "s_cselect_b32 %[so], %[so], 0\n\t"
      "v_mfma_f32_16x16x32_bf16 v[28:31], v[188:191], v[184:187], v[28:31]\n\t"
      "ds_read_b128 v[132:135], v220 offset:1024\n\t"
      "v_mfma_f32_16x16x32_bf16 v[44:47], v[192:195], v[184:187], v[44:47]\n\t"
      "ds_read_b128 v[136:139], v220 offset:2048\n\t"
      "v_mfma_f32_16x16x32_bf16 v[40:43], v[192:195], v[180:183], v[40:43]\n\t"
      "ds_read_b128 v[140:143], v220 offset:3072\n\t"
      "v_mfma_f32_16x16x32_bf16 v[36:39], v[192:195], v[176:179], v[36:39]\n\t"
      "ds_read_b64_tr_b16 v[148:149], v222 offset:32\n\t"
      "v_mfma_f32_16x16x32_bf16 v[32:35], v[192:195], v[172:175], v[32:35]\n\t"
      "ds_read_b64_tr_b16 v[150:151], v222 offset:1824\n\t"
      "v_mfma_f32_16x16x32_bf16 v[48:51], v[196:199], v[172:175], v[48:51]\n\t"
      "ds_read_b64_tr_b16 v[152:153], v221 offset:64\n\t"
      "v_mfma_f32_16x16x32_bf16 v[52:55], v[196:199], v[176:179], v[52:55]\n\t"
      "ds_read_b64_tr_b16 v[154:155], v221 offset:1856\n\t"
      "v_mfma_f32_16x16x32_bf16 v[56:59], v[196:199], v[180:183], v[56:59]\n\t"
      "ds_read_b64_tr_b16 v[156:157], v222 offset:96\n\t"
      "v_mfma_f32_16x16x32_bf16 v[60:63], v[196:199], v[184:187], v[60:63]\n\t"
      "ds_read_b64_tr_b16 v[158:159], v222 offset:1888\n\t"
      "v_mfma_f32_16x16x32_bf16 v[76:79], v[200:203], v[184:187], v[76:79]\n\t"
      "ds_read_b64_tr_b16 v[160:161], v221 offset:128\n\t"
      "v_mfma_f32_16x16x32_bf16 v[72:75], v[200:203], v[180:183], v[72:75]\n\t"
      "ds_read_b64_tr_b16 v[162:163], v221 offset:1920\n\t"
      "v_mfma_f32_16x16x32_bf16 v[68:71], v[200:203], v[176:179], v[68:71]\n\t"
      "ds_read_b64_tr_b16 v[164:165], v222 offset:160\n\t"
      "v_mfma_f32_16x16x32_bf16 v[64:67], v[200:203], v[172:175], v[64:67]\n\t"
      "ds_read_b64_tr_b16 v[166:167], v222 offset:1952\n\t"
      "v_mfma_f32_16x16x32_bf16 v[80:83], v[204:207], v[172:175], v[80:83]\n\t"
      "ds_read_b64_tr_b16 v[168:169], v223 offset:0\n\t"
      "v_mfma_f32_16x16x32_bf16 v[84:87], v[204:207], v[176:179], v[84:87]\n\t"
      "ds_read_b64_tr_b16 v[170:171], v223 offset:1792\n\t"
      "v_mfma_f32_16x16x32_bf16 v[88:91], v[204:207], v[180:183], v[88:91]\n\t"
      "v_add_u32 v220, %[so], v216\n\t"
      "v_add_u32 v221, %[so], v217\n\t"
      "v_mfma_f32_16x16x32_bf16 v[92:95], v[204:207], v[184:187], v[92:95]\n\t"
      "v_add_u32 v222, %[so], v218\n\t"
      "v_add_u32 v223, %[so], v219\n\t"
      "v_mfma_f32_16x16x32_bf16 v[108:111], v[208:211], v[184:187], v[108:111]\n\t"
      "s_add_i32 %[n], %[n], -1\n\t"
      "v_mfma_f32_16x16x32_bf16 v[104:107], v[208:211], v[180:183], v[104:107]\n\t"
      "s_cmp_lg_u32 %[n], 0\n\t"
      "v_mfma_f32_16x16x32_bf16 v[100:103], v[208:211], v[176:179], v[100:103]\n\t"
      "v_mfma_f32_16x16x32_bf16 v[96:99], v[208:211], v[172:175], v[96:99]\n\t"
      "v_mfma_f32_16x16x32_bf16 v[112:115], v[212:215], v[172:175], v[112:115]\n\t"
      "v_mfma_f32_16x16x32_bf16 v[116:119], v[212:215], v[176:179], v[116:119]\n\t"
      "v_mfma_f32_16x16x32_bf16 v[120:123], v[212:215], v[180:183], v[120:123]\n\t"
      "s_waitcnt lgkmcnt(0)\n\t"
      "v_mfma_f32_16x16x32_bf16 v[124:127], v[212:215], v[184:187], v[124:127]\n\t"
      "s_cbranch_scc1 1b\n\t"
      "s_nop 7\n\t"
      "s_nop 7\n\t"
      : "={v[16:47]}"(h0), "={v[48:79]}"(h1), "={v[80:111]}"(h2_), "={v[112:127]}"(h3), [so]"=&s"(so), [n]"=&s"(cnt) SA_OPS
      : "{v216}"(aA), "{v217}"(aE), "{v218}"(aO), "{v219}"(aS), [st]"s"(G1_SLOT), [lim]"s"(G1_R*G1_SLOT) : "memory","scc" SA_CLOB,"v128","v129","v130","v131","v132","v133","v134","v135","v136","v137","v138","v139","v140","v141","v142","v143","v144","v145","v146","v147","v148","v149","v150","v151","v152","v153","v154","v155","v156","v157","v158","v159","v160","v161","v162","v163","v164","v165","v166","v167","v168","v169","v170","v171","v172","v173","v174","v175","v176","v177","v178","v179","v180","v181","v182","v183","v184","v185","v186","v187","v188","v189","v190","v191","v192","v193","v194","v195","v196","v197","v198","v199","v200","v201","v202","v203","v204","v205","v206","v207","v208","v209","v210","v211","v212","v213","v214","v215","v220","v221","v222","v223");
    lane=lane_id(); G=lane>>4; q4=(lane>>2)&3; p4=lane&3;
    STAMP(12);
    rsrc_t rs=mkrs(ws+OFF_HT,(ABL&32)?0:16*HT_PM);
    const unsigned hb=pm*HT_PM+(pj*7*8+wm*4)*1024+lane*16, lh=aX;
    auto act=[&](int j,int i){ const f32x4 v_= j<2? q4of(h0,4*j+i) : j<4? q4of(h1,4*j-8+i) : j<6? q4of(h2_,4*j-16+i) : q4of(h3,i); return silu4(v_.xy,v_.zw); };
    #pragma unroll
    for(int u=0;u<3;u++)
      #pragma unroll
      for(int i=0;i<4;i++){ const u32x4 o=cat(act(2*u,i),act(2*u+1,i));
        __builtin_amdgcn_sched_group_barrier(0x2,4,0); __builtin_amdgcn_sched_group_barrier(0x400,8,0); __builtin_amdgcn_sched_group_barrier(0x2,4,0); __builtin_amdgcn_sched_group_barrier(0x400,8,0); __builtin_amdgcn_sched_group_barrier(0x2,8,0);
        bs128<SC1>(o,rs,hb+wn*32768+i*1024,u*8192); ldsw<u32x4>(lh+wn*65536+u*16384+i*1024,o);
        if(4*u+i==SIGP){ VMC(SIGP-3); if(lane==0) sig(rf,FL_3+(pm*64+pj*4+wid)*128,tag); } }
    #pragma unroll
    for(int i=0;i<4;i++) ldsw<u32x2>(lh+wn*8+3*16384+i*1024,act(6,i));
  }
  STAMP(13);
  { const unsigned aA=aX;
    f32x32 keep,send,frag; unsigned so; int cnt; const unsigned fo=FL_3+131072+(pm*64+pj*4+wid)*128;
    asm volatile("s_mov_b32 %[so], %[st]\n\t"
      "s_mov_b32 %[n], 24\n\t"
      "v_add_u32 v149, %[so], v144\n\t"
      "v_add_u32 v150, %[so], v145\n\t"
      "v_add_u32 v151, %[so], v146\n\t"
      "v_add_u32 v152, %[so], v147\n\t"
      "v_add_u32 v153, %[so], v148\n\t"
      "s_waitcnt lgkmcnt(0)\n\t"
      "s_barrier\n\t"
      SA(14)
      "ds_read_b128 v[80:83], v144 offset:0\n\t"
      "ds_read_b64_tr_b16 v[96:97], v145 offset:0\n\t"
      "ds_read_b64_tr_b16 v[98:99], v145 offset:4096\n\t"
      "ds_read_b128 v[84:87], v144 offset:1024\n\t"
      "ds_read_b128 v[88:91], v144 offset:2048\n\t"
      "ds_read_b128 v[92:95], v144 offset:3072\n\t"
      "ds_read_b64_tr_b16 v[100:101], v146 offset:0\n\t"
      "ds_read_b64_tr_b16 v[102:103], v146 offset:4096\n\t"
      "ds_read_b64_tr_b16 v[104:105], v147 offset:0\n\t"
      "ds_read_b64_tr_b16 v[106:107], v147 offset:4096\n\t"
      "ds_read_b64_tr_b16 v[108:109], v148 offset:0\n\t"
      "ds_read_b64_tr_b16 v[110:111], v148 offset:4096\n\t"
      "s_waitcnt lgkmcnt(0)\n\t"
      "v_mfma_f32_16x16x32_bf16 v[16:19], v[96:99], v[80:83], 0\n\t"
      "ds_read_b128 v[112:115], v149 offset:0\n\t"
      "s_add_i32 %[so], %[so], %[st]\n\t"
      "v_mfma_f32_16x16x32_bf16 v[20:23], v[96:99], v[84:87], 0\n\t"
      "ds_read_b64_tr_b16 v[128:129], v150 offset:0\n\t"
      "s_cmp_lg_u32 %[so], %[lim]\n\t"
      "v_mfma_f32_16x16x32_bf16 v[24:27], v[96:99], v[88:91], 0\n\t"
      "ds_read_b64_tr_b16 v[130:131], v150 offset:4096\n\t"
      "s_cselect_b32 %[so], %[so], 0\n\t"
      "v_mfma_f32_16x16x32_bf16 v[28:31], v[96:99], v[92:95], 0\n\t"
      "ds_read_b128 v[116:119], v149 offset:1024\n\t"
      "v_mfma_f32_16x16x32_bf16 v[44:47], v[100:103], v[92:95], 0\n\t"
      "ds_read_b128 v[120:123], v149 offset:2048\n\t"
      "v_mfma_f32_16x16x32_bf16 v[40:43], v[100:103], v[88:91], 0\n\t"
      "ds_read_b128 v[124:127], v149 offset:3072\n\t"
      "v_mfma_f32_16x16x32_bf16 v[36:39], v[100:103], v[84:87], 0\n\t"
      "ds_read_b64_tr_b16 v[132:133], v151 offset:0\n\t"
      "v_mfma_f32_16x16x32_bf16 v[32:35], v[100:103], v[80:83], 0\n\t"
      "ds_read_b64_tr_b16 v[134:135], v151 offset:4096\n\t"
      "v_mfma_f32_16x16x32_bf16 v[48:51], v[104:107], v[80:83], 0\n\t"
      "ds_read_b64_tr_b16 v[136:137], v152 offset:0\n\t"
      "v_mfma_f32_16x16x32_bf16 v[52:55], v[104:107], v[84:87], 0\n\t"
      "ds_read_b64_tr_b16 v[138:139], v152 offset:4096\n\t"
      "v_mfma_f32_16x16x32_bf16 v[56:59], v[104:107], v[88:91], 0\n\t"
      "ds_read_b64_tr_b16 v[140:141], v153 offset:0\n\t"
      "v_mfma_f32_16x16x32_bf16 v[60:63], v[104:107], v[92:95], 0\n\t"
      "ds_read_b64_tr_b16 v[142:143], v153 offset:4096\n\t"
      "v_mfma_f32_16x16x32_bf16 v[76:79], v[108:111], v[92:95], 0\n\t"
      "v_add_u32 v149, %[so], v144\n\t"
      "v_add_u32 v150, %[so], v145\n\t"
      "v_mfma_f32_16x16x32_bf16 v[72:75], v[108:111], v[88:91], 0\n\t"
      "v_add_u32 v151, %[so], v146\n\t"
      "v_add_u32 v152, %[so], v147\n\t"
      "v_mfma_f32_16x16x32_bf16 v[68:71], v[108:111], v[84:87], 0\n\t"
      "v_add_u32 v153, %[so], v148\n\t"
      "s_cmp_eq_u32 %[n], %[ns]\n\t"
      "s_waitcnt lgkmcnt(0)\n\t"
      "v_mfma_f32_16x16x32_bf16 v[64:67], v[108:111], v[80:83], 0\n\t"
      "s_branch 7f\n\t"
      ".p2align 6\n\t"
      "1:\n\t"
      "v_mfma_f32_16x16x32_bf16 v[16:19], v[96:99], v[80:83], v[16:19]\n\t"
      "ds_read_b128 v[112:115], v149 offset:0\n\t"
      "s_add_i32 %[so], %[so], %[st]\n\t"
      "v_mfma_f32_16x16x32_bf16 v[20:23], v[96:99], v[84:87], v[20:23]\n\t"
      "ds_read_b64_tr_b16 v[128:129], v150 offset:0\n\t"
      "s_cmp_lg_u32 %[so], %[lim]\n\t"
      "v_mfma_f32_16x16x32_bf16 v[24:27], v[96:99], v[88:91], v[24:27]\n\t"
      "ds_read_b64_tr_b16 v[130:131], v150 offset:4096\n\t"
      "s_cselect_b32 %[so], %[so], 0\n\t"
      "v_mfma_f32_16x16x32_bf16 v[28:31], v[96:99], v[92:95], v[28:31]\n\t"
      "ds_read_b128 v[116:119], v149 offset:1024\n\t"
      "v_mfma_f32_16x16x32_bf16 v[44:47], v[100:103], v[92:95], v[44:47]\n\t"
      "ds_read_b128 v[120:123], v149 offset:2048\n\t"
      "v_mfma_f32_16x16x32_bf16 v[40:43], v[100:103], v[88:91], v[40:43]\n\t"
      "ds_read_b128 v[124:127], v149 offset:3072\n\t"
      "v_mfma_f32_16x16x32_bf16 v[36:39], v[100:103], v[84:87], v[36:39]\n\t"
      "ds_read_b64_tr_b16 v[132:133], v151 offset:0\n\t"
      "v_mfma_f32_16x16x32_bf16 v[32:35], v[100:103], v[80:83], v[32:35]\n\t"
      "ds_read_b64_tr_b16 v[134:135], v151 offset:4096\n\t"
      "v_mfma_f32_16x16x32_bf16 v[48:51], v[104:107], v[80:83], v[48:51]\n\t"
      "ds_read_b64_tr_b16 v[136:137], v152 offset:0\n\t"
      "v_mfma_f32_16x16x32_bf16 v[52:55], v[104:107], v[84:87], v[52:55]\n\t"
      "ds_read_b64_tr_b16 v[138:139], v152 offset:4096\n\t"
      "v_mfma_f32_16x16x32_bf16 v[56:59], v[104:107], v[88:91], v[56:59]\n\t"
      "ds_read_b64_tr_b16 v[140:141], v153 offset:0\n\t"
      "v_mfma_f32_16x16x32_bf16 v[60:63], v[104:107], v[92:95], v[60:63]\n\t"
      "ds_read_b64_tr_b16 v[142:143], v153 offset:4096\n\t"
      "v_mfma_f32_16x16x32_bf16 v[76:79], v[108:111], v[92:95], v[76:79]\n\t"
      "v_add_u32 v149, %[so], v144\n\t"
      "v_add_u32 v150, %[so], v145\n\t"
      "v_mfma_f32_16x16x32_bf16 v[72:75], v[108:111], v[88:91], v[72:75]\n\t"
      "v_add_u32 v151, %[so], v146\n\t"
      "v_add_u32 v152, %[so], v147\n\t"
      "v_mfma_f32_16x16x32_bf16 v[68:71], v[108:111], v[84:87], v[68:71]\n\t"
      "v_add_u32 v153, %[so], v148\n\t"
      "s_cmp_eq_u32 %[n], %[ns]\n\t"
      "s_waitcnt lgkmcnt(0)\n\t"
      "v_mfma_f32_16x16x32_bf16 v[64:67], v[108:111], v[80:83], v[64:67]\n\t"
      "7:\n\t"
      "s_barrier\n\t"
      "s_cbranch_scc1 3f\n\t"
      "2:\n\t"
      "v_mfma_f32_16x16x32_bf16 v[16:19], v[128:131], v[112:115], v[16:19]\n\t"
      "ds_read_b128 v[80:83], v149 offset:0\n\t"
      "s_add_i32 %[so], %[so], %[st]\n\t"
      "v_mfma_f32_16x16x32_bf16 v[20:23], v[128:131], v[116:119], v[20:23]\n\t"
      "ds_read_b64_tr_b16 v[96:97], v150 offset:0\n\t"
      "s_cmp_lg_u32 %[so], %[lim]\n\t"
      "v_mfma_f32_16x16x32_bf16 v[24:27], v[128:131], v[120:123], v[24:27]\n\t"
      "ds_read_b64_tr_b16 v[98:99], v150 offset:4096\n\t"
      "s_cselect_b32 %[so], %[so], 0\n\t"
      "v_mfma_f32_16x16x32_bf16 v[28:31], v[128:131], v[124:127], v[28:31]\n\t"
      "ds_read_b128 v[84:87], v149 offset:1024\n\t"
      "v_mfma_f32_16x16x32_bf16 v[44:47], v[132:135], v[124:127], v[44:47]\n\t"
      "ds_read_b128 v[88:91], v149 offset:2048\n\t"
      "v_mfma_f32_16x16x32_bf16 v[40:43], v[132:135], v[120:123], v[40:43]\n\t"
      "ds_read_b128 v[92:95], v149 offset:3072\n\t"
      "v_mfma_f32_16x16x32_bf16 v[36:39], v[132:135], v[116:119], v[36:39]\n\t"
      "ds_read_b64_tr_b16 v[100:101], v151 offset:0\n\t"
      "v_mfma_f32_16x16x32_bf16 v[32:35], v[132:135], v[112:115], v[32:35]\n\t"
      "ds_read_b64_tr_b16 v[102:103], v151 offset:4096\n\t"
      "v_mfma_f32_16x16x32_bf16 v[48:51], v[136:139], v[112:115], v[48:51]\n\t"
      "ds_read_b64_tr_b16 v[104:105], v152 offset:0\n\t"
      "v_mfma_f32_16x16x32_bf16 v[52:55], v[136:139], v[116:119], v[52:55]\n\t"
      "ds_read_b64_tr_b16 v[106:107], v152 offset:4096\n\t"
      "v_mfma_f32_16x16x32_bf16 v[56:59], v[136:139], v[120:123], v[56:59]\n\t"
      "ds_read_b64_tr_b16 v[108:109], v153 offset:0\n\t"
      "v_mfma_f32_16x16x32_bf16 v[60:63], v[136:139], v[124:127], v[60:63]\n\t"
      "ds_read_b64_tr_b16 v[110:111], v153 offset:4096\n\t"
      "v_mfma_f32_16x16x32_bf16 v[76:79], v[140:143], v[124:127], v[76:79]\n\t"
      "v_add_u32 v149, %[so], v144\n\t"
      "v_add_u32 v150, %[so], v145\n\t"
      "v_mfma_f32_16x16x32_bf16 v[72:75], v[140:143], v[120:123], v[72:75]\n\t"
      "v_add_u32 v151, %[so], v146\n\t"
      "v_add_u32 v152, %[so], v147\n\t"
      "s_add_i32 %[n], %[n], -1\n\t"
      "v_mfma_f32_16x16x32_bf16 v[68:71], v[140:143], v[116:119], v[68:71]\n\t"
      "v_add_u32 v153, %[so], v148\n\t"
      "s_cmp_lg_u32 %[n], 0\n\t"
      "s_waitcnt lgkmcnt(0)\n\t"
      "v_mfma_f32_16x16x32_bf16 v[64:67], v[140:143], v[112:115], v[64:67]\n\t"
      "s_cbranch_scc1 1b\n\t"
      "s_branch 4f\n\t"
      "3:\n\t"
      SA(15)
      "s_waitcnt vmcnt(0)\n\t"
      "s_mov_b64 exec, 1\n\t"
      "buffer_store_dword v156, v155, %[rf], 0 offen sc1\n\t"
      "s_mov_b64 exec, -1\n\t"
      SA(16)
      "s_branch 2b\n\t"
      "4:\n\t"
      "s_barrier\n\t"
      SA(17)
      "v_add_u32 v149, 0x10000, v144\n\t"
      "v_add_u32 v150, 0x10000, v147\n\t"
      "v_add_u32 v151, 0x10000, v148\n\t"
      "v_add_u32 v152, 0x20000, v144\n\t"
      "v_add_u32 v153, 0x20000, v147\n\t"
      "v_add_u32 v154, 0x20000, v148\n\t"
      "s_waitcnt lgkmcnt(2)\n\t"
      "v_mfma_f32_16x16x32_bf16 v[48:51], v[104:107], v[80:83], v[48:51]\n\t"
      "ds_read_b128 v[112:115], v149 offset:0\n\t"
      "v_mfma_f32_16x16x32_bf16 v[52:55], v[104:107], v[84:87], v[52:55]\n\t"
      "ds_read_b64_tr_b16 v[136:137], v150 offset:0\n\t"
      "v_mfma_f32_16x16x32_bf16 v[56:59], v[104:107], v[88:91], v[56:59]\n\t"
      "ds_read_b64_tr_b16 v[138:139], v150 offset:4096\n\t"
      "v_mfma_f32_16x16x32_bf16 v[60:63], v[104:107], v[92:95], v[60:63]\n\t"
      "ds_read_b128 v[116:119], v149 offset:1024\n\t"
      "s_waitcnt lgkmcnt(4)\n\t"
      "v_mfma_f32_16x16x32_bf16 v[76:79], v[108:111], v[92:95], v[76:79]\n\t"
      "ds_read_b128 v[120:123], v149 offset:2048\n\t"
      "v_mfma_f32_16x16x32_bf16 v[72:75], v[108:111], v[88:91], v[72:75]\n\t"
      "ds_read_b128 v[124:127], v149 offset:3072\n\t"
      "v_mfma_f32_16x16x32_bf16 v[68:71], v[108:111], v[84:87], v[68:71]\n\t"
      "ds_read_b64_tr_b16 v[140:141], v151 offset:0\n\t"
      "v_mfma_f32_16x16x32_bf16 v[64:67], v[108:111], v[80:83], v[64:67]\n\t"
      "ds_read_b64_tr_b16 v[142:143], v151 offset:4096\n\t"
      "s_waitcnt lgkmcnt(2)\n\t"
      "v_mfma_f32_16x16x32_bf16 v[48:51], v[136:139], v[112:115], v[48:51]\n\t"
      "ds_read_b128 v[80:83], v149 offset:16384\n\t"
      "v_mfma_f32_16x16x32_bf16 v[52:55], v[136:139], v[116:119], v[52:55]\n\t"
      "ds_read_b64_tr_b16 v[104:105], v150 offset:16384\n\t"
      "v_mfma_f32_16x16x32_bf16 v[56:59], v[136:139], v[120:123], v[56:59]\n\t"
      "ds_read_b64_tr_b16 v[106:107], v150 offset:20480\n\t"
      "v_mfma_f32_16x16x32_bf16 v[60:63], v[136:139], v[124:127], v[60:63]\n\t"
      "ds_read_b128 v[84:87], v149 offset:17408\n\t"
      "s_waitcnt lgkmcnt(4)\n\t"
      "v_mfma_f32_16x16x32_bf16 v[76:79], v[140:143], v[124:127], v[76:79]\n\t"
      "ds_read_b128 v[88:91], v149 offset:18432\n\t"
      "v_mfma_f32_16x16x32_bf16 v[72:75], v[140:143], v[120:123], v[72:75]\n\t"
      "ds_read_b128 v[92:95], v149 offset:19456\n\t"
      "v_mfma_f32_16x16x32_bf16 v[68:71], v[140:143], v[116:119], v[68:71]\n\t"
      "ds_read_b64_tr_b16 v[108:109], v151 offset:16384\n\t"
      "v_mfma_f32_16x16x32_bf16 v[64:67], v[140:143], v[112:115], v[64:67]\n\t"
      "ds_read_b64_tr_b16 v[110:111], v151 offset:20480\n\t"
      "s_waitcnt lgkmcnt(2)\n\t"
      "v_mfma_f32_16x16x32_bf16 v[48:51], v[104:107], v[80:83], v[48:51]\n\t"
      "ds_read_b128 v[112:115], v149 offset:32768\n\t"
      "v_mfma_f32_16x16x32_bf16 v[52:55], v[104:107], v[84:87], v[52:55]\n\t"
      "ds_read_b64_tr_b16 v[136:137], v150 offset:32768\n\t"
      "v_mfma_f32_16x16x32_bf16 v[56:59], v[104:107], v[88:91], v[56:59]\n\t"
      "ds_read_b64_tr_b16 v[138:139], v150 offset:36864\n\t"
      "v_mfma_f32_16x16x32_bf16 v[60:63], v[104:107], v[92:95], v[60:63]\n\t"
      "ds_read_b128 v[116:119], v149 offset:33792\n\t"
      "s_waitcnt lgkmcnt(4)\n\t"
      "v_mfma_f32_16x16x32_bf16 v[76:79], v[108:111], v[92:95], v[76:79]\n\t"
      "ds_read_b128 v[120:123], v149 offset:34816\n\t"
      "v_mfma_f32_16x16x32_bf16 v[72:75], v[108:111], v[88:91], v[72:75]\n\t"
      "ds_read_b128 v[124:127], v149 offset:35840\n\t"
      "v_mfma_f32_16x16x32_bf16 v[68:71], v[108:111], v[84:87], v[68:71]\n\t"
      "ds_read_b64_tr_b16 v[140:141], v151 offset:32768\n\t"
      "v_mfma_f32_16x16x32_bf16 v[64:67], v[108:111], v[80:83], v[64:67]\n\t"
      "ds_read_b64_tr_b16 v[142:143], v151 offset:36864\n\t"
      "s_waitcnt lgkmcnt(2)\n\t"
      "v_mfma_f32_16x16x32_bf16 v[48:51], v[136:139], v[112:115], v[48:51]\n\t"
      "ds_read_b128 v[80:83], v149 offset:49152\n\t"
      "v_mfma_f32_16x16x32_bf16 v[52:55], v[136:139], v[116:119], v[52:55]\n\t"
      "ds_read_b64_tr_b16 v[104:105], v150 offset:49152\n\t"
      "v_mfma_f32_16x16x32_bf16 v[56:59], v[136:139], v[120:123], v[56:59]\n\t"
      "ds_read_b64_tr_b16 v[106:107], v150 offset:53248\n\t"
      "v_mfma_f32_16x16x32_bf16 v[60:63], v[136:139], v[124:127], v[60:63]\n\t"
      "ds_read_b128 v[84:87], v149 offset:50176\n\t"
      "s_waitcnt lgkmcnt(4)\n\t"
      "v_mfma_f32_16x16x32_bf16 v[76:79], v[140:143], v[124:127], v[76:79]\n\t"
      "ds_read_b128 v[88:91], v149 offset:51200\n\t"
      "v_mfma_f32_16x16x32_bf16 v[72:75], v[140:143], v[120:123], v[72:75]\n\t"
      "ds_read_b128 v[92:95], v149 offset:52224\n\t"
      "v_mfma_f32_16x16x32_bf16 v[68:71], v[140:143], v[116:119], v[68:71]\n\t"
      "ds_read_b64_tr_b16 v[108:109], v151 offset:49152\n\t"
      "v_mfma_f32_16x16x32_bf16 v[64:67], v[140:143], v[112:115], v[64:67]\n\t"
      "ds_read_b64_tr_b16 v[110:111], v151 offset:53248\n\t"
      "s_barrier\n\t"
      "s_waitcnt lgkmcnt(2)\n\t"
      "v_mfma_f32_16x16x32_bf16 v[48:51], v[104:107], v[80:83], v[48:51]\n\t"
      "ds_read_b128 v[112:115], v152 offset:0\n\t"
      "v_mfma_f32_16x16x32_bf16 v[52:55], v[104:107], v[84:87], v[52:55]\n\t"
      "ds_read_b64_tr_b16 v[136:137], v153 offset:0\n\t"
      "v_mfma_f32_16x16x32_bf16 v[56:59], v[104:107], v[88:91], v[56:59]\n\t"
      "ds_read_b64_tr_b16 v[138:139], v153 offset:4096\n\t"
      "v_mfma_f32_16x16x32_bf16 v[60:63], v[104:107], v[92:95], v[60:63]\n\t"
      "ds_read_b128 v[116:119], v152 offset:1024\n\t"
      "s_waitcnt lgkmcnt(4)\n\t"
      "v_mfma_f32_16x16x32_bf16 v[76:79], v[108:111], v[92:95], v[76:79]\n\t"
      "ds_read_b128 v[120:123], v152 offset:2048\n\t"
      "v_mfma_f32_16x16x32_bf16 v[72:75], v[108:111], v[88:91], v[72:75]\n\t"
      "ds_read_b128 v[124:127], v152 offset:3072\n\t"
      "v_mfma_f32_16x16x32_bf16 v[68:71], v[108:111], v[84:87], v[68:71]\n\t"
      "ds_read_b64_tr_b16 v[140:141], v154 offset:0\n\t"
      "v_mfma_f32_16x16x32_bf16 v[64:67], v[108:111], v[80:83], v[64:67]\n\t"
      "ds_read_b64_tr_b16 v[142:143], v154 offset:4096\n\t"
      "s_waitcnt lgkmcnt(2)\n\t"
      "v_mfma_f32_16x16x32_bf16 v[48:51], v[136:139], v[112:115], v[48:51]\n\t"
      "ds_read_b128 v[80:83], v144 offset:0\n\t"
      "v_mfma_f32_16x16x32_bf16 v[52:55], v[136:139], v[116:119], v[52:55]\n\t"
      "ds_read_b64_tr_b16 v[104:105], v147 offset:0\n\t"
      "v_mfma_f32_16x16x32_bf16 v[56:59], v[136:139], v[120:123], v[56:59]\n\t"
      "ds_read_b64_tr_b16 v[106:107], v147 offset:4096\n\t"
      "v_mfma_f32_16x16x32_bf16 v[60:63], v[136:139], v[124:127], v[60:63]\n\t"
      "ds_read_b128 v[84:87], v144 offset:1024\n\t"
      "s_waitcnt lgkmcnt(4)\n\t"
      "v_mfma_f32_16x16x32_bf16 v[76:79], v[140:143], v[124:127], v[76:79]\n\t"
      "ds_read_b128 v[88:91], v144 offset:2048\n\t"
      "v_mfma_f32_16x16x32_bf16 v[72:75], v[140:143], v[120:123], v[72:75]\n\t"
      "ds_read_b128 v[92:95], v144 offset:3072\n\t"
      "v_mfma_f32_16x16x32_bf16 v[68:71], v[140:143], v[116:119], v[68:71]\n\t"
      "ds_read_b64_tr_b16 v[108:109], v148 offset:0\n\t"
      "v_mfma_f32_16x16x32_bf16 v[64:67], v[140:143], v[112:115], v[64:67]\n\t"
      "ds_read_b64_tr_b16 v[110:111], v148 offset:4096\n\t"
      "s_waitcnt lgkmcnt(2)\n\t"
      "v_mfma_f32_16x16x32_bf16 v[48:51], v[104:107], v[80:83], v[48:51]\n\t"
      "ds_read_b128 v[112:115], v144 offset:16384\n\t"
      "v_mfma_f32_16x16x32_bf16 v[52:55], v[104:107], v[84:87], v[52:55]\n\t"
      "ds_read_b64_tr_b16 v[136:137], v147 offset:16384\n\t"
      "v_mfma_f32_16x16x32_bf16 v[56:59], v[104:107], v[88:91], v[56:59]\n\t"
      "ds_read_b64_tr_b16 v[138:139], v147 offset:20480\n\t"
      "v_mfma_f32_16x16x32_bf16 v[60:63], v[104:107], v[92:95], v[60:63]\n\t"
      "ds_read_b128 v[116:119], v144 offset:17408\n\t"
      "s_waitcnt lgkmcnt(4)\n\t"
      "v_mfma_f32_16x16x32_bf16 v[76:79], v[108:111], v[92:95], v[76:79]\n\t"
      "ds_read_b128 v[120:123], v144 offset:18432\n\t"
      "v_mfma_f32_16x16x32_bf16 v[72:75], v[108:111], v[88:91], v[72:75]\n\t"
      "ds_read_b128 v[124:127], v144 offset:19456\n\t"
      "v_mfma_f32_16x16x32_bf16 v[68:71], v[108:111], v[84:87], v[68:71]\n\t"
      "ds_read_b64_tr_b16 v[140:141], v148 offset:16384\n\t"
      "v_mfma_f32_16x16x32_bf16 v[64:67], v[108:111], v[80:83], v[64:67]\n\t"
      "ds_read_b64_tr_b16 v[142:143], v148 offset:20480\n\t"
      "s_waitcnt lgkmcnt(2)\n\t"
      "v_mfma_f32_16x16x32_bf16 v[48:51], v[136:139], v[112:115], v[48:51]\n\t"
      "ds_read_b128 v[80:83], v144 offset:49152\n\t"
      "v_mfma_f32_16x16x32_bf16 v[52:55], v[136:139], v[116:119], v[52:55]\n\t"
      "ds_read_b64_tr_b16 v[96:97], v145 offset:49152\n\t"
      "v_mfma_f32_16x16x32_bf16 v[56:59], v[136:139], v[120:123], v[56:59]\n\t"
      "ds_read_b64_tr_b16 v[98:99], v145 offset:53248\n\t"
      "v_mfma_f32_16x16x32_bf16 v[60:63], v[136:139], v[124:127], v[60:63]\n\t"
      "ds_read_b128 v[84:87], v144 offset:50176\n\t"
      "s_waitcnt lgkmcnt(4)\n\t"
      "v_mfma_f32_16x16x32_bf16 v[76:79], v[140:143], v[124:127], v[76:79]\n\t"
      "ds_read_b128 v[88:91], v144 offset:51200\n\t"
      "v_mfma_f32_16x16x32_bf16 v[72:75], v[140:143], v[120:123], v[72:75]\n\t"
      "ds_read_b128 v[92:95], v144 offset:52224\n\t"
      "v_mfma_f32_16x16x32_bf16 v[68:71], v[140:143], v[116:119], v[68:71]\n\t"
      "ds_read_b64_tr_b16 v[100:101], v146 offset:49152\n\t"
      "v_mfma_f32_16x16x32_bf16 v[64:67], v[140:143], v[112:115], v[64:67]\n\t"
      "ds_read_b64_tr_b16 v[102:103], v146 offset:53248\n\t"
      "s_waitcnt lgkmcnt(0)\n\t"
      "s_nop 7\n\t"
      "s_nop 7\n\t"
      : "={v[16:47]}"(keep), "={v[48:79]}"(send), "={v[80:111]}"(frag), [so]"=&s"(so), [n]"=&s"(cnt) SA_OPS
      : "{v144}"(aA), "{v145}"(aW[0]), "{v146}"(aW[1]), "{v147}"(aW[2]), "{v148}"(aW[3]), [st]"s"(16384), [lim]"s"(G2_RING), [ns]"i"(24-SIG2), "{v155}"(fo), "{v156}"(tag), [rf]"s"(rf)
      : "memory","scc" SA_CLOB,"v112","v113","v114","v115","v116","v117","v118","v119","v120","v121","v122","v123","v124","v125","v126","v127","v128","v129","v130","v131","v132","v133","v134","v135","v136","v137","v138","v139","v140","v141","v142","v143","v149","v150","v151","v152","v153","v154");
    const int c0=pn*128+kh*64+wn*32; u32x4 pv[2][4];
    rsrc_t rx=mkrs(ws+OFF_XCH,(ABL&16)?0:8u<<20);
    const unsigned me=(pm*8+pn)*2+kh;
    #pragma unroll
    for(int g=0;g<5;g++){ auto v_=[&](int i){ return i<32? send[i] : 0.f; };
      bs128<SC1>((u32x4){cvtpk(v_(7*g),v_(7*g+1)),cvtpk(v_(7*g+2),v_(7*g+3)),cvtpk(v_(7*g+4),v_(7*g+5)),__builtin_amdgcn_perm(tag,cvtpk(v_(7*g+6),0.f),0x05040100)},rx,me*32768+wid*8192+lane*16,g*1024); }
    asm volatile("":::"memory");
    STAMP(18);
    asm volatile("v_add_u32 v149, 0x10000, v144\n\t"
      "v_add_u32 v150, 0x10000, v145\n\t"
      "v_add_u32 v151, 0x10000, v146\n\t"
      "v_add_u32 v152, 0x20000, v144\n\t"
      "v_add_u32 v153, 0x20000, v145\n\t"
      "v_add_u32 v154, 0x20000, v146\n\t"
      "s_waitcnt lgkmcnt(2)\n\t"
      "v_mfma_f32_16x16x32_bf16 v[16:19], v[96:99], v[80:83], v[16:19]\n\t"
      "ds_read_b128 v[112:115], v149 offset:0\n\t"
      "v_mfma_f32_16x16x32_bf16 v[20:23], v[96:99], v[84:87], v[20:23]\n\t"
      "ds_read_b64_tr_b16 v[128:129], v150 offset:0\n\t"
      "v_mfma_f32_16x16x32_bf16 v[24:27], v[96:99], v[88:91], v[24:27]\n\t"
      "ds_read_b64_tr_b16 v[130:131], v150 offset:4096\n\t"
      "v_mfma_f32_16x16x32_bf16 v[28:31], v[96:99], v[92:95], v[28:31]\n\t"
      "ds_read_b128 v[116:119], v149 offset:1024\n\t"
      "s_waitcnt lgkmcnt(4)\n\t"
      "v_mfma_f32_16x16x32_bf16 v[44:47], v[100:103], v[92:95], v[44:47]\n\t"
      "ds_read_b128 v[120:123], v149 offset:2048\n\t"
      "v_mfma_f32_16x16x32_bf16 v[40:43], v[100:103], v[88:91], v[40:43]\n\t"
      "ds_read_b128 v[124:127], v149 offset:3072\n\t"
      "v_mfma_f32_16x16x32_bf16 v[36:39], v[100:103], v[84:87], v[36:39]\n\t"
      "ds_read_b64_tr_b16 v[132:133], v151 offset:0\n\t"
      "v_mfma_f32_16x16x32_bf16 v[32:35], v[100:103], v[80:83], v[32:35]\n\t"
      "ds_read_b64_tr_b16 v[134:135], v151 offset:4096\n\t"
      "s_waitcnt lgkmcnt(2)\n\t"
      "v_mfma_f32_16x16x32_bf16 v[16:19], v[128:131], v[112:115], v[16:19]\n\t"
      "ds_read_b128 v[80:83], v149 offset:16384\n\t"
      "v_mfma_f32_16x16x32_bf16 v[20:23], v[128:131], v[116:119], v[20:23]\n\t"
      "ds_read_b64_tr_b16 v[96:97], v150 offset:16384\n\t"
      "v_mfma_f32_16x16x32_bf16 v[24:27], v[128:131], v[120:123], v[24:27]\n\t"
      "ds_read_b64_tr_b16 v[98:99], v150 offset:20480\n\t"
      "v_mfma_f32_16x16x32_bf16 v[28:31], v[128:131], v[124:127], v[28:31]\n\t"
      "ds_read_b128 v[84:87], v149 offset:17408\n\t"
      "s_waitcnt lgkmcnt(4)\n\t"
      "v_mfma_f32_16x16x32_bf16 v[44:47], v[132:135], v[124:127], v[44:47]\n\t"
      "ds_read_b128 v[88:91], v149 offset:18432\n\t"
      "v_mfma_f32_16x16x32_bf16 v[40:43], v[132:135], v[120:123], v[40:43]\n\t"
      "ds_read_b128 v[92:95], v149 offset:19456\n\t"
      "v_mfma_f32_16x16x32_bf16 v[36:39], v[132:135], v[116:119], v[36:39]\n\t"
      "ds_read_b64_tr_b16 v[100:101], v151 offset:16384\n\t"
      "v_mfma_f32_16x16x32_bf16 v[32:35], v[132:135], v[112:115], v[32:35]\n\t"
      "ds_read_b64_tr_b16 v[102:103], v151 offset:20480\n\t"
      "s_waitcnt lgkmcnt(2)\n\t"
      "v_mfma_f32_16x16x32_bf16 v[16:19], v[96:99], v[80:83], v[16:19]\n\t"
      "ds_read_b128 v[112:115], v149 offset:32768\n\t"
      "v_mfma_f32_16x16x32_bf16 v[20:23], v[96:99], v[84:87], v[20:23]\n\t"
      "ds_read_b64_tr_b16 v[128:129], v150 offset:32768\n\t"
      "v_mfma_f32_16x16x32_bf16 v[24:27], v[96:99], v[88:91], v[24:27]\n\t"
      "ds_read_b64_tr_b16 v[130:131], v150 offset:36864\n\t"
      "v_mfma_f32_16x16x32_bf16 v[28:31], v[96:99], v[92:95], v[28:31]\n\t"
      "ds_read_b128 v[116:119], v149 offset:33792\n\t"
      "s_waitcnt lgkmcnt(4)\n\t"
      "v_mfma_f32_16x16x32_bf16 v[44:47], v[100:103], v[92:95], v[44:47]\n\t"
      "ds_read_b128 v[120:123], v149 offset:34816\n\t"
      "v_mfma_f32_16x16x32_bf16 v[40:43], v[100:103], v[88:91], v[40:43]\n\t"
      "ds_read_b128 v[124:127], v149 offset:35840\n\t"
      "v_mfma_f32_16x16x32_bf16 v[36:39], v[100:103], v[84:87], v[36:39]\n\t"
      "ds_read_b64_tr_b16 v[132:133], v151 offset:32768\n\t"
      "v_mfma_f32_16x16x32_bf16 v[32:35], v[100:103], v[80:83], v[32:35]\n\t"
      "ds_read_b64_tr_b16 v[134:135], v151 offset:36864\n\t"
      "s_waitcnt lgkmcnt(2)\n\t"
      "v_mfma_f32_16x16x32_bf16 v[16:19], v[128:131], v[112:115], v[16:19]\n\t"
      "ds_read_b128 v[80:83], v149 offset:49152\n\t"
      "v_mfma_f32_16x16x32_bf16 v[20:23], v[128:131], v[116:119], v[20:23]\n\t"
      "ds_read_b64_tr_b16 v[96:97], v150 offset:49152\n\t"
      "v_mfma_f32_16x16x32_bf16 v[24:27], v[128:131], v[120:123], v[24:27]\n\t"
      "ds_read_b64_tr_b16 v[98:99], v150 offset:53248\n\t"
      "v_mfma_f32_16x16x32_bf16 v[28:31], v[128:131], v[124:127], v[28:31]\n\t"
      "ds_read_b128 v[84:87], v149 offset:50176\n\t"
      "s_waitcnt lgkmcnt(4)\n\t"
      "v_mfma_f32_16x16x32_bf16 v[44:47], v[132:135], v[124:127], v[44:47]\n\t"
      "ds_read_b128 v[88:91], v149 offset:51200\n\t"
      "v_mfma_f32_16x16x32_bf16 v[40:43], v[132:135], v[120:123], v[40:43]\n\t"
      "ds_read_b128 v[92:95], v149 offset:52224\n\t"
      "v_mfma_f32_16x16x32_bf16 v[36:39], v[132:135], v[116:119], v[36:39]\n\t"
      "ds_read_b64_tr_b16 v[100:101], v151 offset:49152\n\t"
      "v_mfma_f32_16x16x32_bf16 v[32:35], v[132:135], v[112:115], v[32:35]\n\t"
      "ds_read_b64_tr_b16 v[102:103], v151 offset:53248\n\t"
      "s_waitcnt lgkmcnt(2)\n\t"
      "v_mfma_f32_16x16x32_bf16 v[16:19], v[96:99], v[80:83], v[16:19]\n\t"
      "ds_read_b128 v[112:115], v152 offset:0\n\t"
      "v_mfma_f32_16x16x32_bf16 v[20:23], v[96:99], v[84:87], v[20:23]\n\t"
      "ds_read_b64_tr_b16 v[128:129], v153 offset:0\n\t"
      "v_mfma_f32_16x16x32_bf16 v[24:27], v[96:99], v[88:91], v[24:27]\n\t"
      "ds_read_b64_tr_b16 v[130:131], v153 offset:4096\n\t"
      "v_mfma_f32_16x16x32_bf16 v[28:31], v[96:99], v[92:95], v[28:31]\n\t"
      "ds_read_b128 v[116:119], v152 offset:1024\n\t"
      "s_waitcnt lgkmcnt(4)\n\t"
      "v_mfma_f32_16x16x32_bf16 v[44:47], v[100:103], v[92:95], v[44:47]\n\t"
      "ds_read_b128 v[120:123], v152 offset:2048\n\t"
      "v_mfma_f32_16x16x32_bf16 v[40:43], v[100:103], v[88:91], v[40:43]\n\t"
      "ds_read_b128 v[124:127], v152 offset:3072\n\t"
      "v_mfma_f32_16x16x32_bf16 v[36:39], v[100:103], v[84:87], v[36:39]\n\t"
      "ds_read_b64_tr_b16 v[132:133], v154 offset:0\n\t"
      "v_mfma_f32_16x16x32_bf16 v[32:35], v[100:103], v[80:83], v[32:35]\n\t"
      "ds_read_b64_tr_b16 v[134:135], v154 offset:4096\n\t"
      "s_waitcnt lgkmcnt(2)\n\t"
      "v_mfma_f32_16x16x32_bf16 v[16:19], v[128:131], v[112:115], v[16:19]\n\t"
      "ds_read_b128 v[80:83], v144 offset:0\n\t"
      "v_mfma_f32_16x16x32_bf16 v[20:23], v[128:131], v[116:119], v[20:23]\n\t"
      "ds_read_b64_tr_b16 v[96:97], v145 offset:0\n\t"
      "v_mfma_f32_16x16x32_bf16 v[24:27], v[128:131], v[120:123], v[24:27]\n\t"
      "ds_read_b64_tr_b16 v[98:99], v145 offset:4096\n\t"
      "v_mfma_f32_16x16x32_bf16 v[28:31], v[128:131], v[124:127], v[28:31]\n\t"
      "ds_read_b128 v[84:87], v144 offset:1024\n\t"
      "s_waitcnt lgkmcnt(4)\n\t"
      "v_mfma_f32_16x16x32_bf16 v[44:47], v[132:135], v[124:127], v[44:47]\n\t"
      "ds_read_b128 v[88:91], v144 offset:2048\n\t"
      "v_mfma_f32_16x16x32_bf16 v[40:43], v[132:135], v[120:123], v[40:43]\n\t"
      "ds_read_b128 v[92:95], v144 offset:3072\n\t"
      "v_mfma_f32_16x16x32_bf16 v[36:39], v[132:135], v[116:119], v[36:39]\n\t"
      "ds_read_b64_tr_b16 v[100:101], v146 offset:0\n\t"
      "v_mfma_f32_16x16x32_bf16 v[32:35], v[132:135], v[112:115], v[32:35]\n\t"
      "ds_read_b64_tr_b16 v[102:103], v146 offset:4096\n\t"
      "s_waitcnt lgkmcnt(2)\n\t"
      "v_mfma_f32_16x16x32_bf16 v[16:19], v[96:99], v[80:83], v[16:19]\n\t"
      "ds_read_b128 v[112:115], v144 offset:16384\n\t"
      "v_mfma_f32_16x16x32_bf16 v[20:23], v[96:99], v[84:87], v[20:23]\n\t"
      "ds_read_b64_tr_b16 v[128:129], v145 offset:16384\n\t"
      "v_mfma_f32_16x16x32_bf16 v[24:27], v[96:99], v[88:91], v[24:27]\n\t"
      "ds_read_b64_tr_b16 v[130:131], v145 offset:20480\n\t"
      "v_mfma_f32_16x16x32_bf16 v[28:31], v[96:99], v[92:95], v[28:31]\n\t"
      "ds_read_b128 v[116:119], v144 offset:17408\n\t"
      "s_waitcnt lgkmcnt(4)\n\t"
      "v_mfma_f32_16x16x32_bf16 v[44:47], v[100:103], v[92:95], v[44:47]\n\t"
      "ds_read_b128 v[120:123], v144 offset:18432\n\t"
      "v_mfma_f32_16x16x32_bf16 v[40:43], v[100:103], v[88:91], v[40:43]\n\t"
      "ds_read_b128 v[124:127], v144 offset:19456\n\t"
      "v_mfma_f32_16x16x32_bf16 v[36:39], v[100:103], v[84:87], v[36:39]\n\t"
      "ds_read_b64_tr_b16 v[132:133], v146 offset:16384\n\t"
      "v_mfma_f32_16x16x32_bf16 v[32:35], v[100:103], v[80:83], v[32:35]\n\t"
      "ds_read_b64_tr_b16 v[134:135], v146 offset:20480\n\t"
      "s_waitcnt lgkmcnt(2)\n\t"
      "v_mfma_f32_16x16x32_bf16 v[16:19], v[128:131], v[112:115], v[16:19]\n\t"
      "v_mfma_f32_16x16x32_bf16 v[20:23], v[128:131], v[116:119], v[20:23]\n\t"
      "v_mfma_f32_16x16x32_bf16 v[24:27], v[128:131], v[120:123], v[24:27]\n\t"
      "v_mfma_f32_16x16x32_bf16 v[28:31], v[128:131], v[124:127], v[28:31]\n\t"
      "s_waitcnt lgkmcnt(0)\n\t"
      "v_mfma_f32_16x16x32_bf16 v[44:47], v[132:135], v[124:127], v[44:47]\n\t"
      "v_mfma_f32_16x16x32_bf16 v[40:43], v[132:135], v[120:123], v[40:43]\n\t"
      "v_mfma_f32_16x16x32_bf16 v[36:39], v[132:135], v[116:119], v[36:39]\n\t"
      "v_mfma_f32_16x16x32_bf16 v[32:35], v[132:135], v[112:115], v[32:35]\n\t"
      "s_nop 7\n\t"
      "s_nop 7\n\t"
      : "+{v[16:47]}"(keep), "+{v[80:111]}"(frag) : "{v144}"(aA), "{v145}"(aW[0]), "{v146}"(aW[1]) : "memory","v112","v113","v114","v115","v116","v117","v118","v119","v120","v121","v122","v123","v124","v125","v126","v127","v128","v129","v130","v131","v132","v133","v134","v135","v136","v137","v138","v139","v140","v141","v142","v143","v149","v150","v151","v152","v153","v154");
    STAMP(19);
    u32x4 pk[5]; const unsigned xo0=OFF_XCH+(me^1)*32768+wid*8192+lane*16, xo1=xo0+4096;
    #define X_LOADS(tail) asm volatile("buffer_load_dwordx4 %0, %5, %7, 0 offen sc1\n\tbuffer_load_dwordx4 %1, %5, %7, 0 offen offset:1024 sc1\n\tbuffer_load_dwordx4 %2, %5, %7, 0 offen offset:2048 sc1\n\t" \
      "buffer_load_dwordx4 %3, %5, %7, 0 offen offset:3072 sc1\n\tbuffer_load_dwordx4 %4, %6, %7, 0 offen sc1\n\t" tail \
      :"=&v"(pk[0]),"=&v"(pk[1]),"=&v"(pk[2]),"=&v"(pk[3]),"=&v"(pk[4]):"v"(xo0),"v"(xo1),"s"(rf):"memory")
    if(XE) X_LOADS("");
    unsigned oo=(pm*128+wm*64+(lane&15))*2048+(c0+16*(G&1)+8*(G>>1))*2; PIN(oo);
    f32x4 gb[2],t[2][4];
    { u32x4 g0[2],g1[2]; u32x2 xv[2][4]; const unsigned ag=G2_RING+(wn*32+4*G)*4, ax=2*16384+wn*8192+wm*4096+(G>>1)*256+(lane&15)*16+(G&1)*8;
      #pragma unroll
      for(int j=0;j<2;j++){ RD128(g0[j],ag,64*j); RD128(g1[j],ag,1024+64*j);
        #pragma unroll
        for(int i=0;i<4;i++) asm volatile("ds_read_b64 %0, %1 offset:%2":"=v"(xv[j][i]):"v"(ax),"i"(i*1024+j*512)); }
      asm volatile("s_waitcnt lgkmcnt(0)":"+v"(g0[0]),"+v"(g0[1]),"+v"(g1[0]),"+v"(g1[1]),"+v"(xv[0][0]),"+v"(xv[0][1]),"+v"(xv[0][2]),"+v"(xv[0][3]),"+v"(xv[1][0]),"+v"(xv[1][1]),"+v"(xv[1][2]),"+v"(xv[1][3])::"memory");
      #pragma unroll
      for(int j=0;j<2;j++){ const f32x4 m=__builtin_bit_cast(f32x4,g0[j]&0xffffff00u)+__builtin_bit_cast(f32x4,g1[j]&0xffffff00u); const unsigned p0=cvtpk(m[0],m[1]),p1=cvtpk(m[2],m[3]);
        gb[j]=(f32x4){bflo(p0),bfhi(p0),bflo(p1),bfhi(p1)};
        #pragma unroll
        for(int i=0;i<4;i++){ t[j][i]=(f32x4){bflo(xv[j][i].x),bfhi(xv[j][i].x),bflo(xv[j][i].y),bfhi(xv[j][i].y)}+gb[j]*q4of(keep,4*j+i); PIN(t[j][i]); } } }
    if(!XE) X_LOADS("");
    { const u64 t0=__builtin_amdgcn_s_memrealtime();
      asm volatile("s_waitcnt vmcnt(0)":"+v"(pk[0]),"+v"(pk[1]),"+v"(pk[2]),"+v"(pk[3]),"+v"(pk[4])::"memory");
      for(;;){ unsigned m=0;
        #pragma unroll
        for(int g=0;g<5;g++) m|=pk[g].w^(tag<<16);
        if(__all((m>>16)==0) || late(t0)) break;
        X_LOADS("s_waitcnt vmcnt(0)"); }
      #pragma unroll
      for(int i=0;i<32;i++){ const int g=i/7, e=i-7*g; const unsigned d=pk[g][e>>1]; pv[i>>4][(i>>2)&3][i&3]= (e&1)? d&0xffff0000u : d<<16; } }
    STAMP(20);
    rsrc_t ro=mkrs(out,S_*D_*2);
    #pragma unroll
    for(int i=0;i<4;i++){ u32x2 o[2];
      #pragma unroll
      for(int j=0;j<2;j++){ const f32x4 v_=t[j][i]+gb[j]*__builtin_bit_cast(f32x4,pv[j][i]); o[j]=(u32x2){cvtpk(v_[0],v_[1]),cvtpk(v_[2],v_[3])}; }
      auto sx=__builtin_amdgcn_permlane16_swap(o[0].x,o[1].x,false,false); auto sy=__builtin_amdgcn_permlane16_swap(o[0].y,o[1].y,false,false);
      bs128<OUTAUX>((u32x4){sx[0],sy[0],sx[1],sy[1]},ro,oo,i*32768); }
    STAMP(21);
    if(wid==0) STAMP_DUMP(0,24);
  }
}

extern "C" void kernel_launch(void* const* d_in, const int* in_sizes, int n_in, void* d_out, int out_size, void* d_ws, size_t ws_size, hipStream_t stream){
  (void)hipFuncSetAttribute((const void*)k_all,hipFuncAttributeMaxDynamicSharedMemorySize,LDS_BYTES);
  k_all<<<256,512,LDS_BYTES,stream>>>((const u16*)d_in[1],(const u16*)d_in[2],(const u16*)d_in[3],(const u16*)d_in[5],(const u16*)d_in[9],(char*)d_ws,(const u16*)d_in[4],(const u16*)d_in[0],
                                      (const u16*)d_in[7],(const u16*)d_in[8],(const u16*)d_in[10],(u16*)d_out);
}
```
